```python
import torch

def _softmax(x, axis=-1):
    return torch.softmax(x, dim=axis)
def _gelu(x):
    return torch.nn.functional.gelu(x, approximate="tanh")
def _silu(x):
    return torch.nn.functional.silu(x)
def _sigmoid(x):
    return torch.sigmoid(x)
def _layernorm(x, axis=-1, eps=1e-5):
    m = x.mean(dim=axis, keepdim=True)
    v = x.var(dim=axis, keepdim=True, unbiased=False)
    return (x - m) / torch.sqrt(v + eps)
def _scatter_add(tgt, idx, src, axis=0):
    return tgt.clone().index_add_(axis, idx, src)
def _segment_sum(src, seg, n):
    out = torch.zeros((n,) + src.shape[1:], dtype=src.dtype)
    return out.index_add_(0, seg, src)
def _pad(x, pw):
    flat = tuple(v for pair in reversed(pw) for v in pair)
    return torch.nn.functional.pad(x, flat)

kernel_name = 'concat_heads__gqa_block_cached__gqa_block_cached__near_ridge'

def setup_inputs():

    _w = (lambda a: (lambda t: t.to(torch.bfloat16) if t.is_floating_point() else t)(torch.as_tensor(a)))
    def _A_setup():
        B, Sq, D = 16, 128, 576
        Hq, Hkv, Sk, Dh = 32, 16, 1024, 128
        assert Hq % Hkv == 0, (Hq, Hkv)
        return {
            "x": _w((lambda *s: torch.randn(*s, dtype=torch.bfloat16) * 0.8)(B, Sq, D)),
            "k_cache": _w((lambda *s: torch.randn(*s, dtype=torch.bfloat16) * 0.8)(B, Hkv, Sk, Dh)),
            "v_cache": _w((lambda *s: torch.randn(*s, dtype=torch.bfloat16) * 0.05)(B, Hkv, Sk, Dh)),
            "wq": _w((lambda *s: torch.randn(*s, dtype=torch.bfloat16) * 0.8)(D, Hq * Dh) * (D ** -0.5)),
            "wo": _w((lambda *s: torch.randn(*s, dtype=torch.bfloat16) * 0.05)(Hq * Dh, D) * ((Hq * Dh) ** -0.5)),
        }
    _A_in = _A_setup()

    def _B_setup():
        B, Sq, D = 16, 128, 576
        Hq, Hkv, Sk, Dh = 32, 16, 1024, 128
        assert Hq % Hkv == 0, (Hq, Hkv)
        return {
            "x": _w((lambda *s: torch.randn(*s, dtype=torch.bfloat16) * 0.8)(B, Sq, D)),
            "k_cache": _w((lambda *s: torch.randn(*s, dtype=torch.bfloat16) * 0.8)(B, Hkv, Sk, Dh)),
            "v_cache": _w((lambda *s: torch.randn(*s, dtype=torch.bfloat16) * 0.05)(B, Hkv, Sk, Dh)),
            "wq": _w((lambda *s: torch.randn(*s, dtype=torch.bfloat16) * 0.8)(D, Hq * Dh) * (D ** -0.5)),
            "wo": _w((lambda *s: torch.randn(*s, dtype=torch.bfloat16) * 0.05)(Hq * Dh, D) * ((Hq * Dh) ** -0.5)),
        }
    _B_in = _B_setup()

    return {
        "A_x": _A_in["x"],
        "A_k_cache": _A_in["k_cache"],
        "A_v_cache": _A_in["v_cache"],
        "A_wq": _A_in["wq"],
        "A_wo": _A_in["wo"],
        "B_x": _B_in["x"],
        "B_k_cache": _B_in["k_cache"],
        "B_v_cache": _B_in["v_cache"],
        "B_wq": _B_in["wq"],
        "B_wo": _B_in["wo"],
    }

def reference(A_x, A_k_cache, A_v_cache, A_wq, A_wo, B_x, B_k_cache, B_v_cache, B_wq, B_wo):
    def _A_ref(x, k_cache, v_cache, wq, wo):
        B, Sq, D = x.shape
        Hq, Dh = 32, 128
        Hkv = k_cache.shape[1]
        rep = Hq // Hkv
        q = (lambda x, *d: x.permute(*d))((x @ wq).reshape(B, Sq, Hq, Dh), 0, 2, 1, 3)
        k = (lambda x, n, axis: torch.repeat_interleave(x, n, dim=axis))(k_cache, rep, axis=1)
        v = (lambda x, n, axis: torch.repeat_interleave(x, n, dim=axis))(v_cache, rep, axis=1)
        scores = torch.einsum("bhqd,bhkd->bhqk", q, k) * (Dh ** -0.5)
        p = _softmax(scores, axis=-1)
        o = torch.einsum("bhqk,bhkd->bhqd", p, v)
        return (lambda x, *d: x.permute(*d))(o, 0, 2, 1, 3).reshape(B, Sq, Hq * Dh) @ wo

    def _B_ref(x, k_cache, v_cache, wq, wo):
        B, Sq, D = x.shape
        Hq, Dh = 32, 128
        Hkv = k_cache.shape[1]
        rep = Hq // Hkv
        q = (lambda x, *d: x.permute(*d))((x @ wq).reshape(B, Sq, Hq, Dh), 0, 2, 1, 3)
        k = (lambda x, n, axis: torch.repeat_interleave(x, n, dim=axis))(k_cache, rep, axis=1)
        v = (lambda x, n, axis: torch.repeat_interleave(x, n, dim=axis))(v_cache, rep, axis=1)
        scores = torch.einsum("bhqd,bhkd->bhqk", q, k) * (Dh ** -0.5)
        p = _softmax(scores, axis=-1)
        o = torch.einsum("bhqk,bhkd->bhqd", p, v)
        return (lambda x, *d: x.permute(*d))(o, 0, 2, 1, 3).reshape(B, Sq, Hq * Dh) @ wo

    a = _A_ref(A_x, A_k_cache, A_v_cache, A_wq, A_wo)
    b = _B_ref(B_x, B_k_cache, B_v_cache, B_wq, B_wo)
    return torch.concatenate([a, b], axis=-1)
```

```cpp
#include <hip/hip_runtime.h>
#include <hip/hip_bf16.h>
#include <stdint.h>

typedef __attribute__((ext_vector_type(8))) short bf16x8;
typedef __attribute__((ext_vector_type(4))) short s16x4;
typedef __attribute__((ext_vector_type(16))) float f32x16;
typedef __attribute__((ext_vector_type(4))) float f32x4;
typedef __attribute__((ext_vector_type(2))) float f32x2;
typedef __attribute__((ext_vector_type(2))) __bf16 bf16x2;
typedef __attribute__((ext_vector_type(4))) unsigned u32x4;
typedef __attribute__((ext_vector_type(2))) unsigned u32x2;
typedef __attribute__((address_space(3))) s16x4* lds_s16x4_p;
typedef __attribute__((address_space(3))) void* lds_void_p;
typedef const unsigned short* gptr;

#define DEV __device__ __forceinline__

DEV unsigned cvtpk(float lo, float hi) {
  f32x2 v = {lo, hi};
  bf16x2 r = __builtin_convertvector(v, bf16x2);
  return __builtin_bit_cast(unsigned, r);
}
DEV bf16x8 pack8(const f32x16& a, int base, float sc) {
  u32x4 w = {cvtpk(a[base + 0] * sc, a[base + 1] * sc), cvtpk(a[base + 2] * sc, a[base + 3] * sc),
             cvtpk(a[base + 4] * sc, a[base + 5] * sc), cvtpk(a[base + 6] * sc, a[base + 7] * sc)};
  return __builtin_bit_cast(bf16x8, w);
}
DEV bf16x8 pack8n(const f32x16& a, int base) {
  u32x4 w = {cvtpk(a[base + 0], a[base + 1]), cvtpk(a[base + 2], a[base + 3]),
             cvtpk(a[base + 4], a[base + 5]), cvtpk(a[base + 6], a[base + 7])};
  return __builtin_bit_cast(bf16x8, w);
}
DEV void glds(const void* g, char* l) {
  __builtin_amdgcn_global_load_lds((const __attribute__((address_space(1))) void*)g, (lds_void_p)l, 16, 0, 0);
}
DEV s16x4 trrd(const char* l) { return __builtin_amdgcn_ds_read_tr16_b64_v4i16((lds_s16x4_p)l); }
DEV bf16x8 tr2(const char* l, int d) {
  s16x4 a = trrd(l), b = trrd(l + d);
  return __builtin_shufflevector(a, b, 0, 1, 2, 3, 4, 5, 6, 7);
}
DEV bf16x8 ld128(const char* l) { return *reinterpret_cast<const bf16x8*>(l); }
DEV float swap32(float x) {
  auto rr = __builtin_amdgcn_permlane32_swap(__float_as_uint(x), __float_as_uint(x), false, false);
  return __uint_as_float((threadIdx.x & 32) ? rr[0] : rr[1]);
}
#define WAITBAR(n) asm volatile("s_waitcnt vmcnt(" #n ") lgkmcnt(0)\n\ts_barrier" ::: "memory")
#define WAITV(n) asm volatile("s_waitcnt vmcnt(" #n ") lgkmcnt(0)" ::: "memory")

constexpr int SLOT = 32768;
constexpr float QSCALE = 0.088388347648318447f * 1.4426950408889634f;
constexpr float THR = 8.0f;

__global__ __launch_bounds__(512, 2) void attn_kernel(gptr xA, gptr kA, gptr vA, gptr wqA, gptr xB, gptr kB, gptr vB,
                                                      gptr wqB, unsigned short* __restrict__ ows) {
  extern __shared__ __attribute__((aligned(16))) char shm[];
  const int tid = threadIdx.x, lane = tid & 63;
  const int w = __builtin_amdgcn_readfirstlane(tid >> 6);
  const int bid = blockIdx.x, prob = bid >> 8, ii = bid & 255, xcd = ii & 7, jj = ii >> 3;
  const int hkv = 2 * xcd + (jj & 1), b = jj >> 1;
  gptr x = prob ? xB : xA;
  gptr wq = prob ? wqB : wqA;
  gptr Kh = (prob ? kB : kA) + (size_t)(b * 16 + hkv) * (1024 * 128);
  gptr Vh = (prob ? vB : vA) + (size_t)(b * 16 + hkv) * (1024 * 128);
  const int g = w >> 2, tb = w & 3;
  const int h = lane >> 5, l31 = lane & 31;

  const int sub = lane >> 5, row7 = (lane & 31) >> 2, slot4 = lane & 3;
  gptr kg = Kh + (8 * w + row7) * 128 + sub * 32 + ((slot4 ^ ((2 * w + (row7 >> 2)) & 3)) * 8);
  gptr vg = Vh + (8 * w + row7) * 128 + sub * 32 + slot4 * 8;
  gptr wg = wq + (size_t)(8 * (w >> 1) + row7) * 4096 + hkv * 256 + ((2 * w) & 3) * 64 + sub * 32 + slot4 * 8;
  gptr xg = x + (size_t)(b * 128 + 16 * w + (lane >> 2)) * 576 + ((slot4 ^ ((lane >> 4) & 3)) * 8);

  auto issue_kv = [&](int t, int slot) {
    char* dst = shm + slot * SLOT + w * 2048;
    gptr ks = kg + t * 8192;
    gptr vs = vg + t * 8192;
    glds(ks, dst);
    glds(ks + 64, dst + 1024);
    glds(vs, dst + 16384);
    glds(vs + 64, dst + 16384 + 1024);
  };
  auto issue_wx = [&](int c) {
    char* base = shm + 65536 + ((c + 2) & 3) * 24576;
    gptr ws_ = wg + (size_t)c * (32 * 4096);
    glds(ws_, base + w * 2048);
    glds(ws_ + 64, base + w * 2048 + 1024);
    glds(xg + c * 32, base + 16384 + w * 1024);
  };

  issue_wx(0);
  issue_wx(1);
  issue_wx(2);
  issue_kv(0, 0);
  issue_kv(1, 1);

  const int q4 = (lane & 15) >> 2, p4 = lane & 3, blk = (lane >> 4) & 1;
  const int p4s = ((p4 & 1) << 1) | (p4 >> 1);
  const int wbase = h * 4096 + g * 2048 + q4 * 64 + blk * 32 + p4s * 8;
  const int xrow = 32 * tb + l31;
  const int xb0 = 16384 + 512 * (xrow >> 3) + 64 * (xrow & 7) + 16 * ((0 + h) ^ ((xrow >> 2) & 3));
  const int xb1 = 16384 + 512 * (xrow >> 3) + 64 * (xrow & 7) + 16 * ((2 + h) ^ ((xrow >> 2) & 3));

  f32x16 acc[4] = {};
  for (int c = 0; c < 18; ++c) {
    if (c < 3) WAITBAR(14);
    else if (c < 16) WAITBAR(6);
    else if (c == 16) WAITBAR(3);
    else WAITBAR(4);
    if (c < 15) issue_wx(c + 3);
    else if (c == 16) issue_kv(2, 2);
    else if (c == 17) issue_kv(3, 3);
    const char* base = shm + 65536 + ((c + 2) & 3) * 24576;
    bf16x8 xf0 = ld128(base + xb0), xf1 = ld128(base + xb1);
#pragma unroll
    for (int cc = 0; cc < 4; ++cc) {
      bf16x8 a0 = tr2(base + wbase + cc * 512, 256);
      bf16x8 a1 = tr2(base + wbase + 8192 + cc * 512, 256);
      acc[cc] = __builtin_amdgcn_mfma_f32_32x32x16_bf16(a0, xf0, acc[cc], 0, 0, 0);
      acc[cc] = __builtin_amdgcn_mfma_f32_32x32x16_bf16(a1, xf1, acc[cc], 0, 0, 0);
    }
  }
  bf16x8 qf[4][2];
#pragma unroll
  for (int cc = 0; cc < 4; ++cc) {
    qf[cc][0] = pack8(acc[cc], 0, QSCALE);
    qf[cc][1] = pack8(acc[cc], 8, QSCALE);
  }

  const int kb0 = 2048 * (l31 >> 3) + 64 * (l31 & 7) + 16 * ((0 + h) ^ ((l31 >> 2) & 3));
  const int kb1 = 2048 * (l31 >> 3) + 64 * (l31 & 7) + 16 * ((2 + h) ^ ((l31 >> 2) & 3));
  const int vb = 16384 + h * 256 + q4 * 64 + blk * 32 + p4 * 8;
  f32x16 o[4] = {};
  float m = 0.f, l = 0.f;
  f32x16 negm = {};
  int slot = 0;
  for (int j = 0; j < 16; ++j) {
    if (j < 13) WAITBAR(12);
    else if (j == 13) WAITBAR(8);
    else if (j == 14) WAITBAR(4);
    else WAITBAR(0);
    if (j < 12) issue_kv(j + 4, slot == 0 ? 4 : slot - 1);
    const char* tb_ = shm + slot * SLOT;
    f32x16 p0 = negm, p1 = negm;
#pragma unroll
    for (int cc = 0; cc < 4; ++cc) {
      bf16x8 k00 = ld128(tb_ + kb0 + cc * 512), k10 = ld128(tb_ + kb0 + 8192 + cc * 512);
      bf16x8 k01 = ld128(tb_ + kb1 + cc * 512), k11 = ld128(tb_ + kb1 + 8192 + cc * 512);
      p0 = __builtin_amdgcn_mfma_f32_32x32x16_bf16(k00, qf[cc][0], p0, 0, 0, 0);
      p1 = __builtin_amdgcn_mfma_f32_32x32x16_bf16(k10, qf[cc][0], p1, 0, 0, 0);
      p0 = __builtin_amdgcn_mfma_f32_32x32x16_bf16(k01, qf[cc][1], p0, 0, 0, 0);
      p1 = __builtin_amdgcn_mfma_f32_32x32x16_bf16(k11, qf[cc][1], p1, 0, 0, 0);
    }
    float mx = fmaxf(p0[0], p1[0]);
#pragma unroll
    for (int r = 1; r < 16; ++r) mx = fmaxf(fmaxf(mx, p0[r]), p1[r]);
    if (j == 0 || __any(mx > THR)) {
      float rm = fmaxf(mx, swap32(mx));
      float delta = (j == 0) ? rm : fmaxf(rm, 0.f);
      float alpha = (j == 0) ? 1.f : __builtin_amdgcn_exp2f(-delta);
      m += delta;
      l *= alpha;
#pragma unroll
      for (int r = 0; r < 16; ++r) {
        p0[r] -= delta;
        p1[r] -= delta;
        negm[r] = -m;
      }
#pragma unroll
      for (int cc = 0; cc < 4; ++cc)
#pragma unroll
        for (int r = 0; r < 16; ++r) o[cc][r] *= alpha;
    }
    float s0 = 0.f, s1 = 0.f;
#pragma unroll
    for (int r = 0; r < 16; ++r) {
      p0[r] = __builtin_amdgcn_exp2f(p0[r]);
      p1[r] = __builtin_amdgcn_exp2f(p1[r]);
      s0 += p0[r];
      s1 += p1[r];
    }
    l += s0 + s1;
    bf16x8 pf[4] = {pack8n(p0, 0), pack8n(p0, 8), pack8n(p1, 0), pack8n(p1, 8)};
#pragma unroll
    for (int ks = 0; ks < 4; ++ks) {
#pragma unroll
      for (int cc = 0; cc < 4; ++cc) {
        bf16x8 vf = tr2(tb_ + vb + ks * 4096 + cc * 512, 2048);
        o[cc] = __builtin_amdgcn_mfma_f32_32x32x16_bf16(vf, pf[ks], o[cc], 0, 0, 0);
      }
    }
    slot = slot == 4 ? 0 : slot + 1;
  }
  l += swap32(l);
  const float inv = 1.0f / l;
  unsigned short* dst = ows + ((((size_t)(prob * 16 + b) * 4 + tb) * 32 + (2 * hkv + g)) * 8) * 512 + lane * 8;
#pragma unroll
  for (int cc = 0; cc < 4; ++cc) {
    *reinterpret_cast<bf16x8*>(dst + (cc * 2 + 0) * 512) = pack8(o[cc], 0, inv);
    *reinterpret_cast<bf16x8*>(dst + (cc * 2 + 1) * 512) = pack8(o[cc], 8, inv);
  }
}

constexpr int ST2 = 13312;
__global__ __launch_bounds__(256, 1) void oproj_kernel(gptr ows, gptr woA, gptr woB, unsigned short* __restrict__ out) {
  extern __shared__ __attribute__((aligned(16))) char shm[];
  const int tid = threadIdx.x, lane = tid & 63;
  const int w = __builtin_amdgcn_readfirstlane(tid >> 6);
  const int ii = blockIdx.x, xcd = ii & 7, jj = ii >> 3;
  const int prob = xcd >> 2, tbp = (xcd & 3) * 8 + (jj >> 2), cq = jj & 3;
  gptr wo = (prob ? woB : woA) + cq * 144;
  gptr og = ows + ((size_t)(prob * 64 + 2 * tbp) * 32 + 8 * w) * 4096 + lane * 8;
  int goff[9];
#pragma unroll
  for (int pc = 0; pc < 9; ++pc) {
    int st = 4 * pc + (lane >> 4), rg = st / 9, cb = st % 9;
    goff[pc] = (8 * rg + ((lane & 15) >> 1)) * 576 + 16 * cb + 8 * (lane & 1);
  }
  gptr wog = wo + (size_t)(w * 1024) * 576;
  char* my = shm + w * (3 * ST2);
  auto issue = [&](int st, int slot) {
    char* d = my + slot * ST2;
    gptr os = og + st * 1024;
    glds(os, d);
    glds(os + 512, d + 1024);
    glds(os + 32 * 4096, d + 2048);
    glds(os + 32 * 4096 + 512, d + 3072);
    gptr ws_ = wog + (size_t)st * (32 * 576);
#pragma unroll
    for (int pc = 0; pc < 9; ++pc) glds(ws_ + goff[pc], d + 4096 + pc * 1024);
  };
  const int gq = lane >> 4, q4 = (lane & 15) >> 2, p4 = lane & 3;
  const int ob = (gq >> 1) * 1024 + ((gq & 1) * 32 + (lane & 15)) * 16;
  const int ab = 4096 + (gq >> 1) * 4608 + (gq & 1) * 128 + q4 * 32 + p4 * 8;
  f32x4 acc[4][9] = {};
  bf16x8 fa[9], fb[4], ga[9], gb[4];
  auto rd = [&](bf16x8* A, bf16x8* B, int slot) {
    const char* s = my + slot * ST2;
#pragma unroll
    for (int u = 0; u < 4; ++u) B[u] = ld128(s + ob + (u >> 1) * 2048 + (u & 1) * 256);
#pragma unroll
    for (int cb = 0; cb < 9; ++cb) A[cb] = tr2(s + ab + cb * 256, 2304);
  };
  auto mm = [&](bf16x8* A, bf16x8* B) {
#pragma unroll
    for (int cb = 0; cb < 9; ++cb)
#pragma unroll
      for (int u = 0; u < 4; ++u) acc[u][cb] = __builtin_amdgcn_mfma_f32_16x16x32_bf16(A[cb], B[u], acc[u][cb], 0, 0, 0);
  };
  issue(0, 0);
  issue(1, 1);
  issue(2, 2);
  WAITV(26);
  rd(fa, fb, 0);
  int s1 = 1;
  for (int i = 0; i < 32; i += 2) {
    WAITV(13);
    rd(ga, gb, s1);
    if (i + 3 < 32) issue(i + 3, s1 == 0 ? 2 : s1 - 1);
    mm(fa, fb);
    s1 = s1 == 2 ? 0 : s1 + 1;
    if (i + 2 < 32) {
      if (i + 3 < 32) WAITV(13); else WAITV(0);
      rd(fa, fb, s1);
      if (i + 4 < 32) issue(i + 4, s1 == 0 ? 2 : s1 - 1);
    }
    mm(ga, gb);
    s1 = s1 == 2 ? 0 : s1 + 1;
  }
  __syncthreads();
  f32x4* red = reinterpret_cast<f32x4*>(shm);
#pragma unroll
  for (int u = 0; u < 4; ++u)
#pragma unroll
    for (int cb = 0; cb < 9; ++cb) red[((w * 4 + u) * 9 + cb) * 64 + lane] = acc[u][cb];
  __syncthreads();
  const int tok = tbp * 64 + 16 * w + (lane & 15);
  unsigned short* dst = out + (size_t)tok * 1152 + prob * 576 + cq * 144 + 4 * gq;
#pragma unroll
  for (int cb = 0; cb < 9; ++cb) {
    f32x4 s = red[((0 * 4 + w) * 9 + cb) * 64 + lane];
#pragma unroll
    for (int ww = 1; ww < 4; ++ww) s += red[((ww * 4 + w) * 9 + cb) * 64 + lane];
    u32x2 r = {cvtpk(s[0], s[1]), cvtpk(s[2], s[3])};
    *reinterpret_cast<u32x2*>(dst + cb * 16) = r;
  }
}

extern "C" void kernel_launch(void* const* d_in, const int* in_sizes, int n_in, void* d_out, int out_size, void* d_ws,
                              size_t ws_size, hipStream_t stream) {
  (void)in_sizes; (void)n_in; (void)out_size; (void)ws_size;
  gptr xA = (gptr)d_in[0], kA = (gptr)d_in[1], vA = (gptr)d_in[2], wqA = (gptr)d_in[3], woA = (gptr)d_in[4];
  gptr xB = (gptr)d_in[5], kB = (gptr)d_in[6], vB = (gptr)d_in[7], wqB = (gptr)d_in[8], woB = (gptr)d_in[9];
  unsigned short* ows = (unsigned short*)d_ws;
  hipFuncSetAttribute((const void*)attn_kernel, hipFuncAttributeMaxDynamicSharedMemorySize, 163840);
  hipFuncSetAttribute((const void*)oproj_kernel, hipFuncAttributeMaxDynamicSharedMemorySize, 163840);
  hipLaunchKernelGGL(attn_kernel, dim3(512), dim3(512), 163840, stream, xA, kA, vA, wqA, xB, kB, vB, wqB, ows);
  hipLaunchKernelGGL(oproj_kernel, dim3(256), dim3(256), 3 * ST2 * 4, stream, (gptr)ows, woA, woB, (unsigned short*)d_out);
}
```

```cpp
#include <hip/hip_runtime.h>
#include <hip/hip_bf16.h>
#include <stdint.h>

typedef __attribute__((ext_vector_type(8))) short bf16x8;
typedef __attribute__((ext_vector_type(4))) short s16x4;
typedef __attribute__((ext_vector_type(16))) float f32x16;
typedef __attribute__((ext_vector_type(4))) float f32x4;
typedef __attribute__((ext_vector_type(2))) float f32x2;
typedef __attribute__((ext_vector_type(2))) __bf16 bf16x2;
typedef __attribute__((ext_vector_type(4))) unsigned u32x4;
typedef __attribute__((ext_vector_type(2))) unsigned u32x2;
typedef __attribute__((address_space(3))) s16x4* lds_s16x4_p;
typedef __attribute__((address_space(3))) void* lds_void_p;
typedef const unsigned short* gptr;

#define DEV __device__ __forceinline__

DEV unsigned cvtpk(float lo, float hi) {
  f32x2 v = {lo, hi};
  bf16x2 r = __builtin_convertvector(v, bf16x2);
  return __builtin_bit_cast(unsigned, r);
}
DEV bf16x8 pack8(const f32x16& a, int base, float sc) {
  u32x4 w = {cvtpk(a[base + 0] * sc, a[base + 1] * sc), cvtpk(a[base + 2] * sc, a[base + 3] * sc),
             cvtpk(a[base + 4] * sc, a[base + 5] * sc), cvtpk(a[base + 6] * sc, a[base + 7] * sc)};
  return __builtin_bit_cast(bf16x8, w);
}
DEV bf16x8 pack8n(const f32x16& a, int base) {
  u32x4 w = {cvtpk(a[base + 0], a[base + 1]), cvtpk(a[base + 2], a[base + 3]),
             cvtpk(a[base + 4], a[base + 5]), cvtpk(a[base + 6], a[base + 7])};
  return __builtin_bit_cast(bf16x8, w);
}
DEV void glds(unsigned voff, const void* sbase, unsigned lds) {
  unsigned keep;
  asm volatile("s_mov_b32 %0, m0\n\ts_mov_b32 m0, %3\n\ts_nop 0\n\tglobal_load_lds_dwordx4 %1, %2\n\ts_mov_b32 m0, %0"
               : "=&s"(keep) : "v"(voff), "s"(sbase), "s"(lds) : "memory");
}
DEV unsigned ldsaddr(const char* p) { return (unsigned)(size_t)(lds_void_p)p; }
#define SB() __builtin_amdgcn_sched_barrier(0)
DEV s16x4 trrd(const char* l) { return __builtin_amdgcn_ds_read_tr16_b64_v4i16((lds_s16x4_p)l); }
DEV bf16x8 tr2(const char* l, int d) {
  s16x4 a = trrd(l), b = trrd(l + d);
  return __builtin_shufflevector(a, b, 0, 1, 2, 3, 4, 5, 6, 7);
}
DEV bf16x8 ld128(const char* l) { return *reinterpret_cast<const bf16x8*>(l); }
DEV float swap32(float x) {
  auto rr = __builtin_amdgcn_permlane32_swap(__float_as_uint(x), __float_as_uint(x), false, false);
  return __uint_as_float((threadIdx.x & 32) ? rr[0] : rr[1]);
}
#define WAITBAR(n) asm volatile("s_waitcnt vmcnt(" #n ") lgkmcnt(0)\n\ts_barrier" ::: "memory")
#define WAITV(n) asm volatile("s_waitcnt vmcnt(" #n ") lgkmcnt(0)" ::: "memory")

constexpr int SLOT = 32768;
constexpr float QSCALE = 0.088388347648318447f * 1.4426950408889634f;
constexpr float THR = 8.0f;

#define MFMA32(a, b, c) __builtin_amdgcn_mfma_f32_32x32x16_bf16(a, b, c, 0, 0, 0)
__global__ __launch_bounds__(512, 2) void attn_kernel(gptr xA, gptr kA, gptr vA, gptr wqA, gptr xB, gptr kB, gptr vB,
                                                      gptr wqB, unsigned short* __restrict__ ows) {
  extern __shared__ __attribute__((aligned(16))) char shm[];
  const int tid = threadIdx.x, lane = tid & 63;
  const int w = __builtin_amdgcn_readfirstlane(tid >> 6);
  const int ii = blockIdx.x, xcd = ii & 7, jj = ii >> 3;
  const int hkv = 2 * xcd + (jj & 1), b = jj >> 1;
  const int g = w >> 2, tb = w & 3;
  const int h = lane >> 5, l31 = lane & 31;
  const size_t kvoff = (size_t)(b * 16 + hkv) * (1024 * 128);

  const int sub = lane >> 5, row7 = (lane & 31) >> 2, slot4 = lane & 3;
  const unsigned kg = 2 * ((8 * w + row7) * 128 + sub * 32 + ((slot4 ^ ((2 * w + (row7 >> 2)) & 3)) * 8));
  const unsigned vg = 2 * ((8 * w + row7) * 128 + sub * 32 + slot4 * 8);
  const unsigned wg = 2 * ((8 * (w >> 1) + row7) * 4096 + ((2 * w) & 3) * 64 + sub * 32 + slot4 * 8);
  const unsigned xg = 2 * ((16 * w + (lane >> 2)) * 576 + ((slot4 ^ ((lane >> 4) & 3)) * 8));
  const unsigned l0 = ldsaddr(shm);

  auto issue_kv = [&](gptr Kh, gptr Vh, int t, int slot) {
    unsigned dst = l0 + slot * SLOT + w * 2048;
    gptr ks = Kh + t * 8192;
    gptr vs = Vh + t * 8192;
    glds(kg, ks, dst);
    glds(kg, ks + 64, dst + 1024);
    glds(vg, vs, dst + 16384);
    glds(vg, vs + 64, dst + 16384 + 1024);
  };
  auto issue_wx = [&](gptr wqh, gptr xbp, int c, int slot) {
    unsigned base = l0 + slot * SLOT;
    gptr ws_ = wqh + (size_t)c * (32 * 4096);
    glds(wg, ws_, base + w * 2048);
    glds(wg, ws_ + 64, base + w * 2048 + 1024);
    glds(xg, xbp + c * 32, base + 16384 + w * 1024);
  };

  const int q4 = (lane & 15) >> 2, p4 = lane & 3, blk = (lane >> 4) & 1;
  const int p4s = ((p4 & 1) << 1) | (p4 >> 1);
  const int wbase = h * 4096 + g * 2048 + q4 * 64 + blk * 32 + p4s * 8;
  const int xrow = 32 * tb + l31;
  const int xb0 = 16384 + 512 * (xrow >> 3) + 64 * (xrow & 7) + 16 * ((0 + h) ^ ((xrow >> 2) & 3));
  const int xb1 = 16384 + 512 * (xrow >> 3) + 64 * (xrow & 7) + 16 * ((2 + h) ^ ((xrow >> 2) & 3));
  const int kb0 = 2048 * (l31 >> 3) + 64 * (l31 & 7) + 16 * ((0 + h) ^ ((l31 >> 2) & 3));
  const int kb1 = 2048 * (l31 >> 3) + 64 * (l31 & 7) + 16 * ((2 + h) ^ ((l31 >> 2) & 3));
  const int vb = 16384 + h * 256 + q4 * 64 + blk * 32 + p4 * 8;

  int rs = 0, wsl = 4;
#define ADV(x) x = (x == 4 ? 0 : x + 1)
#define SGB(mask, n) __builtin_amdgcn_sched_group_barrier(mask, n, 0)
  {
    gptr wqh = wqA + hkv * 256, xbp = xA + (size_t)b * (128 * 576);
    issue_wx(wqh, xbp, 0, 0);
    issue_wx(wqh, xbp, 1, 1);
    issue_wx(wqh, xbp, 2, 2);
    issue_wx(wqh, xbp, 3, 3);
  }
  gptr nwq = wqB + hkv * 256, nx = xB + (size_t)b * (128 * 576);
  for (int item = 0; item < 2; ++item) {
    gptr wqh = (item ? wqB : wqA) + hkv * 256, xbp = (item ? xB : xA) + (size_t)b * (128 * 576);
    gptr Kh = (item ? kB : kA) + kvoff, Vh = (item ? vB : vA) + kvoff;
    f32x16 acc[4] = {};
    bf16x8 fa[10], fb[10];
    auto qrd = [&](bf16x8* F) {
      const char* base = shm + rs * SLOT;
      F[8] = ld128(base + xb0);
      F[9] = ld128(base + xb1);
#pragma unroll
      for (int cc = 0; cc < 4; ++cc) F[cc] = tr2(base + wbase + cc * 512, 256);
#pragma unroll
      for (int cc = 0; cc < 4; ++cc) F[4 + cc] = tr2(base + wbase + 8192 + cc * 512, 256);
      ADV(rs);
    };
    auto qmm = [&](bf16x8* F) {
#pragma unroll
      for (int cc = 0; cc < 4; ++cc) acc[cc] = MFMA32(F[cc], F[8], acc[cc]);
#pragma unroll
      for (int cc = 0; cc < 4; ++cc) acc[cc] = MFMA32(F[4 + cc], F[9], acc[cc]);
    };
    auto qsched = [&]() {
      SGB(0x100, 4);
#pragma unroll
      for (int i = 0; i < 7; ++i) { SGB(0x8, 1); SGB(0x100, 2); }
      SGB(0x8, 1);
    };
    WAITBAR(9);
    issue_wx(wqh, xbp, 4, wsl); ADV(wsl);
    qrd(fa);
    for (int c = 0; c < 12; c += 2) {
      WAITBAR(9);
      issue_wx(wqh, xbp, c + 5, wsl); ADV(wsl);
      qrd(fb); qmm(fa); qsched(); SB();
      WAITBAR(9);
      issue_wx(wqh, xbp, c + 6, wsl); ADV(wsl);
      qrd(fa); qmm(fb); qsched(); SB();
    }
    WAITBAR(9);
    issue_wx(wqh, xbp, 17, wsl); ADV(wsl);
    qrd(fb); qmm(fa); qsched(); SB();
    for (int c = 0; c < 4; c += 2) {
      WAITBAR(9);
      issue_kv(Kh, Vh, c, wsl); ADV(wsl);
      qrd(fa); qmm(fb); qsched(); SB();
      WAITBAR(9);
      issue_kv(Kh, Vh, c + 1, wsl); ADV(wsl);
      qrd(fb); qmm(fa); qsched(); SB();
    }
    WAITBAR(9);
    issue_kv(Kh, Vh, 4, wsl); ADV(wsl);
    qmm(fb);
    bf16x8 qf[4][2];
#pragma unroll
    for (int cc = 0; cc < 4; ++cc) {
      qf[cc][0] = pack8(acc[cc], 0, QSCALE);
      qf[cc][1] = pack8(acc[cc], 8, QSCALE);
    }

    f32x16 o[4] = {};
    float m = 0.f, l = 0.f;
    f32x16 negm = {};
    f32x16 pc0, pc1, pn0, pn1;
    auto qk = [&](int slot) {
      const char* tb_ = shm + slot * SLOT;
      bf16x8 kf[4][4];
#pragma unroll
      for (int cc = 0; cc < 4; ++cc) {
        kf[cc][0] = ld128(tb_ + kb0 + cc * 512);
        kf[cc][1] = ld128(tb_ + kb0 + 8192 + cc * 512);
        kf[cc][2] = ld128(tb_ + kb1 + cc * 512);
        kf[cc][3] = ld128(tb_ + kb1 + 8192 + cc * 512);
      }
      pn0 = MFMA32(kf[0][0], qf[0][0], negm);
      pn1 = MFMA32(kf[0][1], qf[0][0], negm);
      pn0 = MFMA32(kf[0][2], qf[0][1], pn0);
      pn1 = MFMA32(kf[0][3], qf[0][1], pn1);
#pragma unroll
      for (int cc = 1; cc < 4; ++cc) {
        pn0 = MFMA32(kf[cc][0], qf[cc][0], pn0);
        pn1 = MFMA32(kf[cc][1], qf[cc][0], pn1);
        pn0 = MFMA32(kf[cc][2], qf[cc][1], pn0);
        pn1 = MFMA32(kf[cc][3], qf[cc][1], pn1);
      }
    };
    auto fixmax = [&](bool first) {
      float mx = fmaxf(pn0[0], pn1[0]);
#pragma unroll
      for (int r = 1; r < 16; ++r) mx = fmaxf(fmaxf(mx, pn0[r]), pn1[r]);
      if (first || __any(mx > THR)) {
        SB();
        float rm = fmaxf(mx, swap32(mx));
        float delta = first ? rm : fmaxf(rm, 0.f);
        float alpha = first ? 1.f : __builtin_amdgcn_exp2f(-delta);
        m += delta;
        l *= alpha;
#pragma unroll
        for (int r = 0; r < 16; ++r) {
          pn0[r] -= delta;
          pn1[r] -= delta;
          negm[r] = -m;
        }
#pragma unroll
        for (int cc = 0; cc < 4; ++cc)
#pragma unroll
          for (int r = 0; r < 16; ++r) o[cc][r] *= alpha;
        SB();
      }
      pc0 = pn0;
      pc1 = pn1;
    };
    auto astep = [&](auto doqk, auto dma) {
      const char* tb_ = shm + rs * SLOT;
      ADV(rs);
      if (doqk) qk(rs);
#pragma unroll
      for (int r = 0; r < 16; ++r) {
        pc0[r] = __builtin_amdgcn_exp2f(pc0[r]);
        pc1[r] = __builtin_amdgcn_exp2f(pc1[r]);
      }
      bf16x8 pf[4] = {pack8n(pc0, 0), pack8n(pc0, 8), pack8n(pc1, 0), pack8n(pc1, 8)};
      bf16x8 vf[4][4];
#pragma unroll
      for (int cc = 0; cc < 4; ++cc) vf[0][cc] = tr2(tb_ + vb + cc * 512, 2048);
      if (doqk) {
        SGB(0x100, 8);
        SGB(0x2, 6);
#pragma unroll
        for (int i = 0; i < 8; ++i) { SGB(0x8, 1); SGB(0x100, 1); SGB(0x2, 3); }
#pragma unroll
        for (int i = 0; i < 8; ++i) { SGB(0x8, 1); SGB(0x100, 1); SGB(0x2, 3); }
      }
      SB();
      dma();
#pragma unroll
      for (int ks = 1; ks < 4; ++ks)
#pragma unroll
        for (int cc = 0; cc < 4; ++cc) vf[ks][cc] = tr2(tb_ + vb + ks * 4096 + cc * 512, 2048);
#pragma unroll
      for (int ks = 0; ks < 4; ++ks)
#pragma unroll
        for (int cc = 0; cc < 4; ++cc) o[cc] = MFMA32(vf[ks][cc], pf[ks], o[cc]);
      float s0 = 0.f, s1 = 0.f;
#pragma unroll
      for (int r = 0; r < 16; ++r) {
        s0 += pc0[r];
        s1 += pc1[r];
      }
      l += s0 + s1;
      if (doqk) {
        float mx = fmaxf(pn0[0], pn1[0]);
#pragma unroll
        for (int r = 1; r < 16; ++r) mx = fmaxf(fmaxf(mx, pn0[r]), pn1[r]);
        asm volatile("" : "+v"(mx));
#pragma unroll
        for (int i = 0; i < 12; ++i) { SGB(0x8, 1); SGB(0x100, 2); SGB(0x2, 2); }
#pragma unroll
        for (int i = 0; i < 4; ++i) { SGB(0x8, 1); SGB(0x2, 3); }
        SB();
        if (__any(mx > THR)) {
          float rm = fmaxf(mx, swap32(mx));
          float delta = fmaxf(rm, 0.f);
          float alpha = __builtin_amdgcn_exp2f(-delta);
          m += delta;
          l *= alpha;
#pragma unroll
          for (int r = 0; r < 16; ++r) {
            pn0[r] -= delta;
            pn1[r] -= delta;
            negm[r] = -m;
          }
#pragma unroll
          for (int cc = 0; cc < 4; ++cc)
#pragma unroll
            for (int r = 0; r < 16; ++r) o[cc][r] *= alpha;
        }
        pc0 = pn0;
        pc1 = pn1;
      }
    };
    struct T_ { constexpr operator bool() const { return true; } };
    struct F_ { constexpr operator bool() const { return false; } };
    qk(rs);
    fixmax(true);
    WAITBAR(12);
    astep(T_{}, [&]() {});
    for (int j = 1; j < 12; ++j) {
      WAITBAR(8);
      astep(T_{}, [&]() { issue_kv(Kh, Vh, j + 4, wsl); ADV(wsl); });
    }
    for (int j = 0; j < 3; ++j) {
      if (j == 0) WAITBAR(8);
      else if (j == 1) WAITBAR(4);
      else WAITBAR(0);
      astep(T_{}, [&]() { if (item == 0) issue_wx(nwq, nx, j, wsl); ADV(wsl); });
    }
    WAITBAR(0);
    astep(F_{}, [&]() { if (item == 0) issue_wx(nwq, nx, 3, wsl); ADV(wsl); });
    l += swap32(l);
    const float inv = 1.0f / l;
    unsigned short* dst = ows + ((((size_t)(item * 16 + b) * 4 + tb) * 32 + (2 * hkv + g)) * 8) * 512 + lane * 8;
#pragma unroll
    for (int cc = 0; cc < 4; ++cc) {
      *reinterpret_cast<bf16x8*>(dst + (cc * 2 + 0) * 512) = pack8(o[cc], 0, inv);
      *reinterpret_cast<bf16x8*>(dst + (cc * 2 + 1) * 512) = pack8(o[cc], 8, inv);
    }
  }
}

constexpr int ST2 = 13312;
__global__ __launch_bounds__(512, 2) void oproj_kernel(gptr ows, gptr woA, gptr woB, unsigned short* __restrict__ out) {
  extern __shared__ __attribute__((aligned(16))) char shm[];
  const int tid = threadIdx.x, lane = tid & 63;
  const int w = __builtin_amdgcn_readfirstlane(tid >> 6);
  const int kq = w & 3, th = w >> 2;
  const int ii = blockIdx.x, xcd = ii & 7, jj = ii >> 3;
  const int prob = xcd >> 2, tbp = (xcd & 3) * 8 + (jj >> 2), cq = jj & 3;
  gptr wo = (prob ? woB : woA) + cq * 144;
  gptr og = ows + ((size_t)(prob * 64 + 2 * tbp + th) * 32 + 8 * kq) * 4096;
  const unsigned ol = lane * 16;
  unsigned goff[5];
#pragma unroll
  for (int pc = 0; pc < 5; ++pc) {
    int st = 4 * (pc + 4 * th) + (lane >> 4), rg = st / 9, cb = st % 9;
    goff[pc] = 2 * ((8 * rg + ((lane & 15) >> 1)) * 576 + 16 * cb + 8 * (lane & 1));
  }
  gptr wog = wo + (size_t)(kq * 1024) * 576;
  char* my = shm + kq * (3 * ST2);
  const unsigned myl = ldsaddr(my);
  auto issue = [&](int st, int slot) {
    unsigned d = myl + slot * ST2;
    gptr os = og + st * 1024;
    glds(ol, os, d + th * 2048);
    glds(ol, os + 512, d + th * 2048 + 1024);
    gptr ws_ = wog + (size_t)st * (32 * 576);
#pragma unroll
    for (int pc = 0; pc < 5; ++pc) glds(goff[pc], ws_, d + 4096 + th * 4096 + pc * 1024);
  };
  const int gq = lane >> 4, q4 = (lane & 15) >> 2, p4 = lane & 3;
  const int ob = th * 2048 + (gq >> 1) * 1024 + ((gq & 1) * 32 + (lane & 15)) * 16;
  const int ab = 4096 + (gq >> 1) * 4608 + (gq & 1) * 128 + q4 * 32 + p4 * 8;
  f32x4 acc[2][9] = {};
  issue(0, 0);
  issue(1, 1);
  int slot = 0;
  for (int i = 0; i < 32; ++i) {
    WAITBAR(7);
    issue(min(i + 2, 31), slot == 0 ? 2 : slot - 1);
    const char* s = my + slot * ST2;
    bf16x8 A[9], B[2];
    B[0] = ld128(s + ob);
    B[1] = ld128(s + ob + 256);
#pragma unroll
    for (int cb = 0; cb < 9; ++cb) A[cb] = tr2(s + ab + cb * 256, 2304);
    SB();
#pragma unroll
    for (int cb = 0; cb < 9; ++cb) {
      acc[0][cb] = __builtin_amdgcn_mfma_f32_16x16x32_bf16(A[cb], B[0], acc[0][cb], 0, 0, 0);
      acc[1][cb] = __builtin_amdgcn_mfma_f32_16x16x32_bf16(A[cb], B[1], acc[1][cb], 0, 0, 0);
    }
    slot = slot == 2 ? 0 : slot + 1;
  }
  WAITBAR(0);
  f32x4* red = reinterpret_cast<f32x4*>(shm);
#pragma unroll
  for (int u = 0; u < 2; ++u)
#pragma unroll
    for (int cb = 0; cb < 9; ++cb) red[((kq * 4 + th * 2 + u) * 9 + cb) * 64 + lane] = acc[u][cb];
  __syncthreads();
  const int u = w >> 1, c0 = (w & 1) * 5, c1 = (w & 1) ? 9 : 5;
  const int tok = tbp * 64 + 16 * u + (lane & 15);
  unsigned short* dst = out + (size_t)tok * 1152 + prob * 576 + cq * 144 + 4 * gq;
  for (int cb = c0; cb < c1; ++cb) {
    f32x4 s = red[((0 * 4 + u) * 9 + cb) * 64 + lane];
#pragma unroll
    for (int ww = 1; ww < 4; ++ww) s += red[((ww * 4 + u) * 9 + cb) * 64 + lane];
    u32x2 r = {cvtpk(s[0], s[1]), cvtpk(s[2], s[3])};
    *reinterpret_cast<u32x2*>(dst + cb * 16) = r;
  }
}

extern "C" void kernel_launch(void* const* d_in, const int* in_sizes, int n_in, void* d_out, int out_size, void* d_ws,
                              size_t ws_size, hipStream_t stream) {
  (void)in_sizes; (void)n_in; (void)out_size; (void)ws_size;
  gptr xA = (gptr)d_in[0], kA = (gptr)d_in[1], vA = (gptr)d_in[2], wqA = (gptr)d_in[3], woA = (gptr)d_in[4];
  gptr xB = (gptr)d_in[5], kB = (gptr)d_in[6], vB = (gptr)d_in[7], wqB = (gptr)d_in[8], woB = (gptr)d_in[9];
  unsigned short* ows = (unsigned short*)d_ws;
  hipFuncSetAttribute((const void*)attn_kernel, hipFuncAttributeMaxDynamicSharedMemorySize, 163840);
  hipFuncSetAttribute((const void*)oproj_kernel, hipFuncAttributeMaxDynamicSharedMemorySize, 163840);
  hipLaunchKernelGGL(attn_kernel, dim3(256), dim3(512), 163840, stream, xA, kA, vA, wqA, xB, kB, vB, wqB, ows);
  hipLaunchKernelGGL(oproj_kernel, dim3(256), dim3(512), 3 * ST2 * 4, stream, (gptr)ows, woA, woB, (unsigned short*)d_out);
}
```

```cpp
#include <hip/hip_runtime.h>
#include <hip/hip_bf16.h>
#include <stdint.h>

typedef __attribute__((ext_vector_type(8))) short bf16x8;
typedef __attribute__((ext_vector_type(4))) short s16x4;
typedef __attribute__((ext_vector_type(16))) float f32x16;
typedef __attribute__((ext_vector_type(4))) float f32x4;
typedef __attribute__((ext_vector_type(2))) float f32x2;
typedef __attribute__((ext_vector_type(2))) __bf16 bf16x2;
typedef __attribute__((ext_vector_type(4))) unsigned u32x4;
typedef __attribute__((ext_vector_type(2))) unsigned u32x2;
typedef __attribute__((address_space(3))) s16x4* lds_s16x4_p;
typedef __attribute__((address_space(3))) void* lds_void_p;
typedef const unsigned short* gptr;

#define DEV __device__ __forceinline__

DEV unsigned cvtpk(float lo, float hi) {
  f32x2 v = {lo, hi};
  bf16x2 r = __builtin_convertvector(v, bf16x2);
  return __builtin_bit_cast(unsigned, r);
}
DEV bf16x8 pack8(const f32x16& a, int base, float sc) {
  u32x4 w = {cvtpk(a[base + 0] * sc, a[base + 1] * sc), cvtpk(a[base + 2] * sc, a[base + 3] * sc),
             cvtpk(a[base + 4] * sc, a[base + 5] * sc), cvtpk(a[base + 6] * sc, a[base + 7] * sc)};
  return __builtin_bit_cast(bf16x8, w);
}
DEV bf16x8 pack8n(const f32x16& a, int base) {
  u32x4 w = {cvtpk(a[base + 0], a[base + 1]), cvtpk(a[base + 2], a[base + 3]),
             cvtpk(a[base + 4], a[base + 5]), cvtpk(a[base + 6], a[base + 7])};
  return __builtin_bit_cast(bf16x8, w);
}
DEV void glds(unsigned voff, const void* sbase, unsigned lds) {
  unsigned keep;
  asm volatile("s_mov_b32 %0, m0\n\ts_mov_b32 m0, %3\n\ts_nop 0\n\tglobal_load_lds_dwordx4 %1, %2\n\ts_mov_b32 m0, %0"
               : "=&s"(keep) : "v"(voff), "s"(sbase), "s"(lds) : "memory");
}
DEV unsigned ldsaddr(const char* p) { return (unsigned)(size_t)(lds_void_p)p; }
#define SB() __builtin_amdgcn_sched_barrier(0)
DEV s16x4 trrd(const char* l) { return __builtin_amdgcn_ds_read_tr16_b64_v4i16((lds_s16x4_p)l); }
DEV bf16x8 tr2(const char* l, int d) {
  s16x4 a = trrd(l), b = trrd(l + d);
  return __builtin_shufflevector(a, b, 0, 1, 2, 3, 4, 5, 6, 7);
}
DEV bf16x8 ld128(const char* l) { return *reinterpret_cast<const bf16x8*>(l); }
DEV float swap32(float x) {
  auto rr = __builtin_amdgcn_permlane32_swap(__float_as_uint(x), __float_as_uint(x), false, false);
  return __uint_as_float((threadIdx.x & 32) ? rr[0] : rr[1]);
}
#define WAITBAR(n) asm volatile("s_waitcnt vmcnt(" #n ") lgkmcnt(0)\n\ts_barrier" ::: "memory")
#define WAITV(n) asm volatile("s_waitcnt vmcnt(" #n ") lgkmcnt(0)" ::: "memory")

constexpr int SLOT = 32768;
constexpr float QSCALE = 0.088388347648318447f * 1.4426950408889634f;
constexpr float THR = 8.0f;

#define MFMA32(a, b, c) __builtin_amdgcn_mfma_f32_32x32x16_bf16(a, b, c, 0, 0, 0)
__global__ __launch_bounds__(512, 2) void attn_kernel(gptr xA, gptr kA, gptr vA, gptr wqA, gptr xB, gptr kB, gptr vB,
                                                      gptr wqB, unsigned short* __restrict__ ows) {
  extern __shared__ __attribute__((aligned(16))) char shm[];
  const int tid = threadIdx.x, lane = tid & 63;
  const int w = __builtin_amdgcn_readfirstlane(tid >> 6);
  const int ii = blockIdx.x, xcd = ii & 7, jj = ii >> 3;
  const int hkv = 2 * xcd + (jj & 1), b = jj >> 1;
  const int g = w >> 2, tb = w & 3;
  const int h = lane >> 5, l31 = lane & 31;
  const size_t kvoff = (size_t)(b * 16 + hkv) * (1024 * 128);

  const int sub = lane >> 5, row7 = (lane & 31) >> 2, slot4 = lane & 3;
  const unsigned kg = 2 * ((8 * w + row7) * 128 + sub * 32 + ((slot4 ^ ((2 * w + (row7 >> 2)) & 3)) * 8));
  const unsigned vg = 2 * ((8 * w + row7) * 128 + sub * 32 + slot4 * 8);
  const unsigned wg = 2 * ((8 * (w >> 1) + row7) * 4096 + ((2 * w) & 3) * 64 + sub * 32 + slot4 * 8);
  const unsigned xg = 2 * ((16 * w + (lane >> 2)) * 576 + ((slot4 ^ ((lane >> 4) & 3)) * 8));
  const unsigned l0 = ldsaddr(shm);

  auto piece_kv = [&](gptr Kh, gptr Vh, int t, int slot, int pc) {
    unsigned dst = l0 + slot * SLOT + w * 2048;
    if (pc == 0) glds(kg, Kh + t * 8192, dst);
    if (pc == 1) glds(kg, Kh + t * 8192 + 64, dst + 1024);
    if (pc == 2) glds(vg, Vh + t * 8192, dst + 16384);
    if (pc == 3) glds(vg, Vh + t * 8192 + 64, dst + 16384 + 1024);
  };
  auto piece_wx = [&](gptr wqh, gptr xbp, int c, int slot, int pc) {
    unsigned base = l0 + slot * SLOT;
    gptr ws_ = wqh + (size_t)c * (32 * 4096);
    if (pc == 0) glds(wg, ws_, base + w * 2048);
    if (pc == 1) glds(wg, ws_ + 64, base + w * 2048 + 1024);
    if (pc == 2) glds(xg, xbp + c * 32, base + 16384 + w * 1024);
  };
  auto issue_wx = [&](gptr wqh, gptr xbp, int c, int slot) {
    piece_wx(wqh, xbp, c, slot, 0);
    piece_wx(wqh, xbp, c, slot, 1);
    piece_wx(wqh, xbp, c, slot, 2);
  };

  const int q4 = (lane & 15) >> 2, p4 = lane & 3, blk = (lane >> 4) & 1;
  const int p4s = ((p4 & 1) << 1) | (p4 >> 1);
  const int wbase = h * 4096 + g * 2048 + q4 * 64 + blk * 32 + p4s * 8;
  const int xrow = 32 * tb + l31;
  const int xb0 = 16384 + 512 * (xrow >> 3) + 64 * (xrow & 7) + 16 * ((0 + h) ^ ((xrow >> 2) & 3));
  const int xb1 = 16384 + 512 * (xrow >> 3) + 64 * (xrow & 7) + 16 * ((2 + h) ^ ((xrow >> 2) & 3));
  const int kb0 = 2048 * (l31 >> 3) + 64 * (l31 & 7) + 16 * ((0 + h) ^ ((l31 >> 2) & 3));
  const int kb1 = 2048 * (l31 >> 3) + 64 * (l31 & 7) + 16 * ((2 + h) ^ ((l31 >> 2) & 3));
  const int vb = 16384 + h * 256 + q4 * 64 + blk * 32 + p4 * 8;

  int rs = 0, wsl = 4;
#define ADV(x) x = (x == 4 ? 0 : x + 1)
  struct T_ { constexpr operator bool() const { return true; } };
  struct F_ { constexpr operator bool() const { return false; } };
  {
    gptr wqh = wqA + hkv * 256, xbp = xA + (size_t)b * (128 * 576);
    issue_wx(wqh, xbp, 0, 0);
    issue_wx(wqh, xbp, 1, 1);
    issue_wx(wqh, xbp, 2, 2);
    issue_wx(wqh, xbp, 3, 3);
  }
  gptr nwq = wqB + hkv * 256, nx = xB + (size_t)b * (128 * 576);
  for (int item = 0; item < 2; ++item) {
    gptr wqh = (item ? wqB : wqA) + hkv * 256, xbp = (item ? xB : xA) + (size_t)b * (128 * 576);
    gptr Kh = (item ? kB : kA) + kvoff, Vh = (item ? vB : vA) + kvoff;
    f32x16 acc[4] = {};
    bf16x8 fa[10], fb[10];
    auto qstep = [&](bf16x8* Fn, bf16x8* Fc, auto domm, auto dma) {
      const char* base = shm + rs * SLOT;
      ADV(rs);
#pragma unroll
      for (int i = 0; i < 8; ++i) {
        if (domm) acc[i & 3] = MFMA32(Fc[i], Fc[8 + (i >> 2)], acc[i & 3]);
        if (i == 0) {
          Fn[8] = ld128(base + xb0);
          Fn[9] = ld128(base + xb1);
        }
        Fn[i] = tr2(base + wbase + (i >> 2) * 8192 + (i & 3) * 512, 256);
        if (i & 1) dma(i >> 1);
        SB();
      }
      ADV(wsl);
    };
    WAITBAR(9);
    qstep(fa, fb, F_{}, [&](int pc) { piece_wx(wqh, xbp, 4, wsl, pc); });
    for (int c = 0; c < 12; c += 2) {
      WAITBAR(9);
      qstep(fb, fa, T_{}, [&](int pc) { piece_wx(wqh, xbp, c + 5, wsl, pc); });
      WAITBAR(9);
      qstep(fa, fb, T_{}, [&](int pc) { piece_wx(wqh, xbp, c + 6, wsl, pc); });
    }
    WAITBAR(9);
    qstep(fb, fa, T_{}, [&](int pc) { piece_wx(wqh, xbp, 17, wsl, pc); });
    for (int c = 0; c < 4; c += 2) {
      WAITBAR(9);
      qstep(fa, fb, T_{}, [&](int pc) { piece_kv(Kh, Vh, c, wsl, pc); });
      WAITBAR(9);
      qstep(fb, fa, T_{}, [&](int pc) { piece_kv(Kh, Vh, c + 1, wsl, pc); });
    }
    WAITBAR(9);
#pragma unroll
    for (int i = 0; i < 8; ++i) {
      acc[i & 3] = MFMA32(fb[i], fb[8 + (i >> 2)], acc[i & 3]);
      if (i & 1) piece_kv(Kh, Vh, 4, wsl, i >> 1);
      SB();
    }
    ADV(wsl);
    bf16x8 qf[4][2];
#pragma unroll
    for (int cc = 0; cc < 4; ++cc) {
      qf[cc][0] = pack8(acc[cc], 0, QSCALE);
      qf[cc][1] = pack8(acc[cc], 8, QSCALE);
    }

    f32x16 o[4] = {};
    float m, l = 0.f;
    f32x16 negm = {};
    f32x16 A0, A1, B0, B1;
    {
      const char* kp0 = shm + rs * SLOT + kb0;
      const char* kp1 = shm + rs * SLOT + kb1;
      bf16x8 kf[16];
#pragma unroll
      for (int i = 0; i < 16; ++i) kf[i] = ld128(((i & 2) ? kp1 : kp0) + (i & 1) * 8192 + (i >> 2) * 512);
#pragma unroll
      for (int i = 0; i < 16; ++i) {
        if (i & 1) A1 = MFMA32(kf[i], qf[i >> 2][(i >> 1) & 1], i < 2 ? negm : A1);
        else A0 = MFMA32(kf[i], qf[i >> 2][(i >> 1) & 1], i < 2 ? negm : A0);
      }
      float mx = fmaxf(A0[0], A1[0]);
#pragma unroll
      for (int r = 1; r < 16; ++r) mx = fmaxf(fmaxf(mx, A0[r]), A1[r]);
      m = fmaxf(mx, swap32(mx));
#pragma unroll
      for (int r = 0; r < 16; ++r) {
        A0[r] -= m;
        A1[r] -= m;
        negm[r] = -m;
      }
      SB();
    }
    auto astep = [&](f32x16& c0, f32x16& c1, f32x16& n0, f32x16& n1, auto doqk, auto dma) {
      const char* vbp = shm + rs * SLOT + vb;
      ADV(rs);
      const char* kp0 = shm + rs * SLOT + kb0;
      const char* kp1 = shm + rs * SLOT + kb1;
      bf16x8 kf[16], vf[16];
      u32x4 pw[4];
      if (doqk) {
#pragma unroll
        for (int i = 0; i < 4; ++i) kf[i] = ld128(((i & 2) ? kp1 : kp0) + (i & 1) * 8192 + (i >> 2) * 512);
      }
#pragma unroll
      for (int r = 0; r < 4; ++r) c0[r] = __builtin_amdgcn_exp2f(c0[r]);
      SB();
#pragma unroll
      for (int i = 0; i < 16; ++i) {
        if (doqk) {
          if (i & 1) n1 = MFMA32(kf[i], qf[i >> 2][(i >> 1) & 1], i < 2 ? negm : n1);
          else n0 = MFMA32(kf[i], qf[i >> 2][(i >> 1) & 1], i < 2 ? negm : n0);
          if (i < 12) kf[i + 4] = ld128((((i + 4) & 2) ? kp1 : kp0) + (i & 1) * 8192 + ((i + 4) >> 2) * 512);
        }
        if (i < 6) {
          c0[4 + 2 * i] = __builtin_amdgcn_exp2f(c0[4 + 2 * i]);
          c0[5 + 2 * i] = __builtin_amdgcn_exp2f(c0[5 + 2 * i]);
        } else if (i < 14) {
          c1[2 * i - 12] = __builtin_amdgcn_exp2f(c1[2 * i - 12]);
          c1[2 * i - 11] = __builtin_amdgcn_exp2f(c1[2 * i - 11]);
        }
        pw[i >> 2][i & 3] = i < 8 ? cvtpk(c0[2 * i], c0[2 * i + 1]) : cvtpk(c1[2 * i - 16], c1[2 * i - 15]);
        if (i >= 12) vf[i - 12] = tr2(vbp + (i - 12) * 512, 2048);
        SB();
      }
      float s0 = c0[0], s1 = c1[0], mx = 0.f;
#pragma unroll
      for (int i = 0; i < 16; ++i) {
        o[i & 3] = MFMA32(vf[i], __builtin_bit_cast(bf16x8, pw[i >> 2]), o[i & 3]);
        if (i < 12) vf[i + 4] = tr2(vbp + ((i + 4) >> 2) * 4096 + (i & 3) * 512, 2048);
        if (i > 0) {
          asm volatile("v_add_f32_e32 %0, %0, %1" : "+v"(s0) : "v"(c0[i]));
          asm volatile("v_add_f32_e32 %0, %0, %1" : "+v"(s1) : "v"(c1[i]));
        }
        if (doqk) {
          if (i == 1) mx = fmaxf(n0[0], n1[0]);
          if (i >= 1) mx = fmaxf(fmaxf(mx, n0[i]), n1[i]);
        }
        if ((i & 3) == 1) dma(i >> 2);
        SB();
      }
      ADV(wsl);
      l += s0 + s1;
      if (doqk) {
        if (__any(mx > THR)) {
          float rm = fmaxf(mx, swap32(mx));
          float delta = fmaxf(rm, 0.f);
          float alpha = __builtin_amdgcn_exp2f(-delta);
          m += delta;
          l *= alpha;
#pragma unroll
          for (int r = 0; r < 16; ++r) {
            n0[r] -= delta;
            n1[r] -= delta;
            negm[r] = -m;
          }
#pragma unroll
          for (int cc = 0; cc < 4; ++cc)
#pragma unroll
            for (int r = 0; r < 16; ++r) o[cc][r] *= alpha;
        }
      }
    };
    WAITBAR(12);
    wsl = wsl == 0 ? 4 : wsl - 1;
    astep(A0, A1, B0, B1, T_{}, [&](int) {});
    for (int j = 1; j < 11; j += 2) {
      WAITBAR(8);
      astep(B0, B1, A0, A1, T_{}, [&](int pc) { piece_kv(Kh, Vh, j + 4, wsl, pc); });
      WAITBAR(8);
      astep(A0, A1, B0, B1, T_{}, [&](int pc) { piece_kv(Kh, Vh, j + 5, wsl, pc); });
    }
    WAITBAR(8);
    astep(B0, B1, A0, A1, T_{}, [&](int pc) { piece_kv(Kh, Vh, 15, wsl, pc); });
    WAITBAR(8);
    astep(A0, A1, B0, B1, T_{}, [&](int pc) { if (item == 0) piece_wx(nwq, nx, 0, wsl, pc); });
    WAITBAR(4);
    astep(B0, B1, A0, A1, T_{}, [&](int pc) { if (item == 0) piece_wx(nwq, nx, 1, wsl, pc); });
    WAITBAR(0);
    astep(A0, A1, B0, B1, T_{}, [&](int pc) { if (item == 0) piece_wx(nwq, nx, 2, wsl, pc); });
    WAITBAR(0);
    astep(B0, B1, A0, A1, F_{}, [&](int pc) { if (item == 0) piece_wx(nwq, nx, 3, wsl, pc); });
    l += swap32(l);
    const float inv = 1.0f / l;
    unsigned short* dst = ows + ((((size_t)(item * 16 + b) * 4 + tb) * 32 + (2 * hkv + g)) * 8) * 512 + lane * 8;
#pragma unroll
    for (int cc = 0; cc < 4; ++cc) {
      *reinterpret_cast<bf16x8*>(dst + (cc * 2 + 0) * 512) = pack8(o[cc], 0, inv);
      *reinterpret_cast<bf16x8*>(dst + (cc * 2 + 1) * 512) = pack8(o[cc], 8, inv);
    }
  }
}

constexpr int ST2 = 13312;
__global__ __launch_bounds__(512, 2) void oproj_kernel(gptr ows, gptr woA, gptr woB, unsigned short* __restrict__ out) {
  extern __shared__ __attribute__((aligned(16))) char shm[];
  const int tid = threadIdx.x, lane = tid & 63;
  const int w = __builtin_amdgcn_readfirstlane(tid >> 6);
  const int kq = w & 3, th = w >> 2;
  const int ii = blockIdx.x, xcd = ii & 7, jj = ii >> 3;
  const int prob = xcd >> 2, tbp = (xcd & 3) * 8 + (jj >> 2), cq = jj & 3;
  gptr wo = (prob ? woB : woA) + cq * 144;
  gptr og = ows + ((size_t)(prob * 64 + 2 * tbp + th) * 32 + 8 * kq) * 4096;
  const unsigned ol = lane * 16;
  unsigned goff[5];
#pragma unroll
  for (int pc = 0; pc < 5; ++pc) {
    int st = 4 * (pc + 4 * th) + (lane >> 4), rg = st / 9, cb = st % 9;
    goff[pc] = 2 * ((8 * rg + ((lane & 15) >> 1)) * 576 + 16 * cb + 8 * (lane & 1));
  }
  gptr wog = wo + (size_t)(kq * 1024) * 576;
  char* my = shm + kq * (3 * ST2);
  const unsigned myl = ldsaddr(my);
  auto issue = [&](int st, int slot) {
    unsigned d = myl + slot * ST2;
    gptr os = og + st * 1024;
    glds(ol, os, d + th * 2048);
    glds(ol, os + 512, d + th * 2048 + 1024);
    gptr ws_ = wog + (size_t)st * (32 * 576);
#pragma unroll
    for (int pc = 0; pc < 5; ++pc) glds(goff[pc], ws_, d + 4096 + th * 4096 + pc * 1024);
  };
  const int gq = lane >> 4, q4 = (lane & 15) >> 2, p4 = lane & 3;
  const int ob = th * 2048 + (gq >> 1) * 1024 + ((gq & 1) * 32 + (lane & 15)) * 16;
  const int ab = 4096 + (gq >> 1) * 4608 + (gq & 1) * 128 + q4 * 32 + p4 * 8;
  f32x4 acc[2][9] = {};
  issue(0, 0);
  issue(1, 1);
  int slot = 0;
  for (int i = 0; i < 32; ++i) {
    WAITBAR(7);
    issue(min(i + 2, 31), slot == 0 ? 2 : slot - 1);
    const char* s = my + slot * ST2;
    bf16x8 A[9], B[2];
    B[0] = ld128(s + ob);
    B[1] = ld128(s + ob + 256);
#pragma unroll
    for (int cb = 0; cb < 9; ++cb) A[cb] = tr2(s + ab + cb * 256, 2304);
    SB();
#pragma unroll
    for (int cb = 0; cb < 9; ++cb) {
      acc[0][cb] = __builtin_amdgcn_mfma_f32_16x16x32_bf16(A[cb], B[0], acc[0][cb], 0, 0, 0);
      acc[1][cb] = __builtin_amdgcn_mfma_f32_16x16x32_bf16(A[cb], B[1], acc[1][cb], 0, 0, 0);
    }
    slot = slot == 2 ? 0 : slot + 1;
  }
  WAITBAR(0);
  f32x4* red = reinterpret_cast<f32x4*>(shm);
#pragma unroll
  for (int u = 0; u < 2; ++u)
#pragma unroll
    for (int cb = 0; cb < 9; ++cb) red[((kq * 4 + th * 2 + u) * 9 + cb) * 64 + lane] = acc[u][cb];
  __syncthreads();
  const int u = w >> 1, c0 = (w & 1) * 5, c1 = (w & 1) ? 9 : 5;
  const int tok = tbp * 64 + 16 * u + (lane & 15);
  unsigned short* dst = out + (size_t)tok * 1152 + prob * 576 + cq * 144 + 4 * gq;
  for (int cb = c0; cb < c1; ++cb) {
    f32x4 s = red[((0 * 4 + u) * 9 + cb) * 64 + lane];
#pragma unroll
    for (int ww = 1; ww < 4; ++ww) s += red[((ww * 4 + u) * 9 + cb) * 64 + lane];
    u32x2 r = {cvtpk(s[0], s[1]), cvtpk(s[2], s[3])};
    *reinterpret_cast<u32x2*>(dst + cb * 16) = r;
  }
}

extern "C" void kernel_launch(void* const* d_in, const int* in_sizes, int n_in, void* d_out, int out_size, void* d_ws,
                              size_t ws_size, hipStream_t stream) {
  (void)in_sizes; (void)n_in; (void)out_size; (void)ws_size;
  gptr xA = (gptr)d_in[0], kA = (gptr)d_in[1], vA = (gptr)d_in[2], wqA = (gptr)d_in[3], woA = (gptr)d_in[4];
  gptr xB = (gptr)d_in[5], kB = (gptr)d_in[6], vB = (gptr)d_in[7], wqB = (gptr)d_in[8], woB = (gptr)d_in[9];
  unsigned short* ows = (unsigned short*)d_ws;
  hipFuncSetAttribute((const void*)attn_kernel, hipFuncAttributeMaxDynamicSharedMemorySize, 163840);
  hipFuncSetAttribute((const void*)oproj_kernel, hipFuncAttributeMaxDynamicSharedMemorySize, 163840);
  hipLaunchKernelGGL(attn_kernel, dim3(256), dim3(512), 163840, stream, xA, kA, vA, wqA, xB, kB, vB, wqB, ows);
  hipLaunchKernelGGL(oproj_kernel, dim3(256), dim3(512), 3 * ST2 * 4, stream, (gptr)ows, woA, woB, (unsigned short*)d_out);
}
```

```cpp
#include <hip/hip_runtime.h>
#include <hip/hip_bf16.h>
#include <stdint.h>

typedef __attribute__((ext_vector_type(8))) short bf16x8;
typedef __attribute__((ext_vector_type(4))) short s16x4;
typedef __attribute__((ext_vector_type(16))) float f32x16;
typedef __attribute__((ext_vector_type(4))) float f32x4;
typedef __attribute__((ext_vector_type(2))) float f32x2;
typedef __attribute__((ext_vector_type(2))) __bf16 bf16x2;
typedef __attribute__((ext_vector_type(4))) unsigned u32x4;
typedef __attribute__((ext_vector_type(2))) unsigned u32x2;
typedef __attribute__((address_space(3))) s16x4* lds_s16x4_p;
typedef __attribute__((address_space(3))) void* lds_void_p;
typedef const unsigned short* gptr;

#define DEV __device__ __forceinline__

DEV unsigned cvtpk(float lo, float hi) {
  f32x2 v = {lo, hi};
  bf16x2 r = __builtin_convertvector(v, bf16x2);
  return __builtin_bit_cast(unsigned, r);
}
DEV bf16x8 pack8(const f32x16& a, int base, float sc) {
  u32x4 w = {cvtpk(a[base + 0] * sc, a[base + 1] * sc), cvtpk(a[base + 2] * sc, a[base + 3] * sc),
             cvtpk(a[base + 4] * sc, a[base + 5] * sc), cvtpk(a[base + 6] * sc, a[base + 7] * sc)};
  return __builtin_bit_cast(bf16x8, w);
}
DEV bf16x8 pack8n(const f32x16& a, int base) {
  u32x4 w = {cvtpk(a[base + 0], a[base + 1]), cvtpk(a[base + 2], a[base + 3]),
             cvtpk(a[base + 4], a[base + 5]), cvtpk(a[base + 6], a[base + 7])};
  return __builtin_bit_cast(bf16x8, w);
}
DEV void glds(unsigned voff, const void* sbase, unsigned lds) {
  unsigned keep;
  asm volatile("s_mov_b32 %0, m0\n\ts_mov_b32 m0, %3\n\ts_nop 0\n\tglobal_load_lds_dwordx4 %1, %2\n\ts_mov_b32 m0, %0"
               : "=&s"(keep) : "v"(voff), "s"(sbase), "s"(lds) : "memory");
}
DEV unsigned ldsaddr(const char* p) { return (unsigned)(size_t)(lds_void_p)p; }
#define SB() __builtin_amdgcn_sched_barrier(0)
DEV s16x4 trrd(const char* l) { return __builtin_amdgcn_ds_read_tr16_b64_v4i16((lds_s16x4_p)l); }
DEV bf16x8 tr2(const char* l, int d) {
  s16x4 a = trrd(l), b = trrd(l + d);
  return __builtin_shufflevector(a, b, 0, 1, 2, 3, 4, 5, 6, 7);
}
DEV bf16x8 ld128(const char* l) { return *reinterpret_cast<const bf16x8*>(l); }
DEV float swap32(float x) {
  auto rr = __builtin_amdgcn_permlane32_swap(__float_as_uint(x), __float_as_uint(x), false, false);
  return __uint_as_float((threadIdx.x & 32) ? rr[0] : rr[1]);
}
#define WAITBAR(n) asm volatile("s_waitcnt vmcnt(" #n ") lgkmcnt(0)\n\ts_barrier" ::: "memory")

constexpr int SLOT = 32768;
constexpr float QSCALE = 0.088388347648318447f * 1.4426950408889634f;
constexpr float THR = 8.0f;

#define MFMA32(a, b, c) __builtin_amdgcn_mfma_f32_32x32x16_bf16(a, b, c, 0, 0, 0)
__global__ __launch_bounds__(512, 2) void attn_kernel(gptr xA, gptr kA, gptr vA, gptr wqA, gptr xB, gptr kB, gptr vB,
                                                      gptr wqB, unsigned short* __restrict__ ows) {
  extern __shared__ __attribute__((aligned(16))) char shm[];
  const int tid = threadIdx.x, lane = tid & 63;
  const int w = __builtin_amdgcn_readfirstlane(tid >> 6);
  const int ii = blockIdx.x, xcd = ii & 7, jj = ii >> 3;
  const int hkv = 2 * xcd + (jj & 1), b = jj >> 1;
  const int g = w >> 2, tb = w & 3;
  const int h = lane >> 5, l31 = lane & 31;
  const size_t kvoff = (size_t)(b * 16 + hkv) * (1024 * 128);

  const int sub = lane >> 5, row7 = (lane & 31) >> 2, slot4 = lane & 3;
  const unsigned kg = 2 * ((8 * w + row7) * 128 + sub * 32 + ((slot4 ^ ((2 * w + (row7 >> 2)) & 3)) * 8));
  const unsigned vg = 2 * ((8 * w + row7) * 128 + sub * 32 + slot4 * 8);
  const unsigned wg = 2 * ((8 * (w >> 1) + row7) * 4096 + ((2 * w) & 3) * 64 + sub * 32 + slot4 * 8);
  const unsigned xg = 2 * ((16 * w + (lane >> 2)) * 576 + ((slot4 ^ ((lane >> 4) & 3)) * 8));
  const unsigned l0 = ldsaddr(shm);

  auto piece_kv = [&](gptr Kh, gptr Vh, int t, int slot, int pc) {
    unsigned dst = l0 + slot * SLOT + w * 2048;
    if (pc == 0) glds(kg, Kh + t * 8192, dst);
    if (pc == 1) glds(kg, Kh + t * 8192 + 64, dst + 1024);
    if (pc == 2) glds(vg, Vh + t * 8192, dst + 16384);
    if (pc == 3) glds(vg, Vh + t * 8192 + 64, dst + 16384 + 1024);
  };
  auto piece_wx = [&](gptr wqh, gptr xbp, int c, int slot, int pc) {
    unsigned base = l0 + slot * SLOT;
    gptr ws_ = wqh + (size_t)c * (32 * 4096);
    if (pc == 0) glds(wg, ws_, base + w * 2048);
    if (pc == 1) glds(wg, ws_ + 64, base + w * 2048 + 1024);
    if (pc == 2) glds(xg, xbp + c * 32, base + 16384 + w * 1024);
  };
  auto issue_wx = [&](gptr wqh, gptr xbp, int c, int slot) {
    piece_wx(wqh, xbp, c, slot, 0);
    piece_wx(wqh, xbp, c, slot, 1);
    piece_wx(wqh, xbp, c, slot, 2);
  };

  const int q4 = (lane & 15) >> 2, p4 = lane & 3, blk = (lane >> 4) & 1;
  const int p4s = ((p4 & 1) << 1) | (p4 >> 1);
  const int wbase = h * 4096 + g * 2048 + q4 * 64 + blk * 32 + p4s * 8;
  const int xrow = 32 * tb + l31;
  const int xb0 = 16384 + 512 * (xrow >> 3) + 64 * (xrow & 7) + 16 * ((0 + h) ^ ((xrow >> 2) & 3));
  const int xb1 = 16384 + 512 * (xrow >> 3) + 64 * (xrow & 7) + 16 * ((2 + h) ^ ((xrow >> 2) & 3));
  const int kb0 = 2048 * (l31 >> 3) + 64 * (l31 & 7) + 16 * ((0 + h) ^ ((l31 >> 2) & 3));
  const int kb1 = 2048 * (l31 >> 3) + 64 * (l31 & 7) + 16 * ((2 + h) ^ ((l31 >> 2) & 3));
  const int vb = 16384 + h * 256 + q4 * 64 + blk * 32 + p4 * 8;

  int rs = 0, wsl = 4;
#define ADV(x) x = (x == 4 ? 0 : x + 1)
  struct T_ { constexpr operator bool() const { return true; } };
  struct F_ { constexpr operator bool() const { return false; } };
  {
    gptr wqh = wqA + hkv * 256, xbp = xA + (size_t)b * (128 * 576);
    issue_wx(wqh, xbp, 0, 0);
    issue_wx(wqh, xbp, 1, 1);
    issue_wx(wqh, xbp, 2, 2);
    issue_wx(wqh, xbp, 3, 3);
  }
  gptr nwq = wqB + hkv * 256, nx = xB + (size_t)b * (128 * 576);
  for (int item = 0; item < 2; ++item) {
    gptr wqh = (item ? wqB : wqA) + hkv * 256, xbp = (item ? xB : xA) + (size_t)b * (128 * 576);
    gptr Kh = (item ? kB : kA) + kvoff, Vh = (item ? vB : vA) + kvoff;
    f32x16 acc[4] = {};
    bf16x8 fa[10], fb[10];
    auto qstep = [&](bf16x8* Fn, bf16x8* Fc, auto domm, auto dma) {
      const char* base = shm + rs * SLOT;
      ADV(rs);
#pragma unroll
      for (int i = 0; i < 8; ++i) {
        if (domm) acc[i & 3] = MFMA32(Fc[i], Fc[8 + (i >> 2)], acc[i & 3]);
        if (i == 0) {
          Fn[8] = ld128(base + xb0);
          Fn[9] = ld128(base + xb1);
        }
        Fn[i] = tr2(base + wbase + (i >> 2) * 8192 + (i & 3) * 512, 256);
        if (i & 1) dma(i >> 1);
        SB();
      }
      ADV(wsl);
    };
    WAITBAR(9);
    qstep(fa, fb, F_{}, [&](int pc) { piece_wx(wqh, xbp, 4, wsl, pc); });
    for (int c = 0; c < 12; c += 2) {
      WAITBAR(9);
      qstep(fb, fa, T_{}, [&](int pc) { piece_wx(wqh, xbp, c + 5, wsl, pc); });
      WAITBAR(9);
      qstep(fa, fb, T_{}, [&](int pc) { piece_wx(wqh, xbp, c + 6, wsl, pc); });
    }
    WAITBAR(9);
    qstep(fb, fa, T_{}, [&](int pc) { piece_wx(wqh, xbp, 17, wsl, pc); });
    for (int c = 0; c < 4; c += 2) {
      WAITBAR(9);
      qstep(fa, fb, T_{}, [&](int pc) { piece_kv(Kh, Vh, c, wsl, pc); });
      WAITBAR(9);
      qstep(fb, fa, T_{}, [&](int pc) { piece_kv(Kh, Vh, c + 1, wsl, pc); });
    }
    WAITBAR(9);
#pragma unroll
    for (int i = 0; i < 8; ++i) {
      acc[i & 3] = MFMA32(fb[i], fb[8 + (i >> 2)], acc[i & 3]);
      if (i & 1) piece_kv(Kh, Vh, 4, wsl, i >> 1);
      SB();
    }
    ADV(wsl);
    bf16x8 qf[4][2];
#pragma unroll
    for (int cc = 0; cc < 4; ++cc) {
      qf[cc][0] = pack8(acc[cc], 0, QSCALE);
      qf[cc][1] = pack8(acc[cc], 8, QSCALE);
    }

    f32x16 o[4] = {};
    float m, l = 0.f;
    f32x16 negm = {};
    f32x16 A0, A1, B0, B1;
    {
      const char* kp0 = shm + rs * SLOT + kb0;
      const char* kp1 = shm + rs * SLOT + kb1;
      bf16x8 kf[16];
#pragma unroll
      for (int i = 0; i < 16; ++i) kf[i] = ld128(((i & 2) ? kp1 : kp0) + (i & 1) * 8192 + (i >> 2) * 512);
#pragma unroll
      for (int i = 0; i < 16; ++i) {
        if (i & 1) A1 = MFMA32(kf[i], qf[i >> 2][(i >> 1) & 1], i < 2 ? negm : A1);
        else A0 = MFMA32(kf[i], qf[i >> 2][(i >> 1) & 1], i < 2 ? negm : A0);
      }
      float mx = fmaxf(A0[0], A1[0]);
#pragma unroll
      for (int r = 1; r < 16; ++r) mx = fmaxf(fmaxf(mx, A0[r]), A1[r]);
      m = fmaxf(mx, swap32(mx));
#pragma unroll
      for (int r = 0; r < 16; ++r) {
        A0[r] -= m;
        A1[r] -= m;
        negm[r] = -m;
      }
      SB();
    }
    auto astep = [&](f32x16& c0, f32x16& c1, f32x16& n0, f32x16& n1, auto doqk, auto dma) {
      const char* vbp = shm + rs * SLOT + vb;
      ADV(rs);
      const char* kp0 = shm + rs * SLOT + kb0;
      const char* kp1 = shm + rs * SLOT + kb1;
      bf16x8 kf[16], vf[16];
      u32x4 pw[4];
      if (doqk) {
#pragma unroll
        for (int i = 0; i < 4; ++i) kf[i] = ld128(((i & 2) ? kp1 : kp0) + (i & 1) * 8192 + (i >> 2) * 512);
      }
#pragma unroll
      for (int r = 0; r < 4; ++r) c0[r] = __builtin_amdgcn_exp2f(c0[r]);
      SB();
#pragma unroll
      for (int i = 0; i < 16; ++i) {
        if (doqk) {
          if (i & 1) n1 = MFMA32(kf[i], qf[i >> 2][(i >> 1) & 1], i < 2 ? negm : n1);
          else n0 = MFMA32(kf[i], qf[i >> 2][(i >> 1) & 1], i < 2 ? negm : n0);
          if (i < 12) kf[i + 4] = ld128((((i + 4) & 2) ? kp1 : kp0) + (i & 1) * 8192 + ((i + 4) >> 2) * 512);
        }
        if (i < 6) {
          c0[4 + 2 * i] = __builtin_amdgcn_exp2f(c0[4 + 2 * i]);
          c0[5 + 2 * i] = __builtin_amdgcn_exp2f(c0[5 + 2 * i]);
        } else if (i < 14) {
          c1[2 * i - 12] = __builtin_amdgcn_exp2f(c1[2 * i - 12]);
          c1[2 * i - 11] = __builtin_amdgcn_exp2f(c1[2 * i - 11]);
        }
        pw[i >> 2][i & 3] = i < 8 ? cvtpk(c0[2 * i], c0[2 * i + 1]) : cvtpk(c1[2 * i - 16], c1[2 * i - 15]);
        if (i >= 12) vf[i - 12] = tr2(vbp + (i - 12) * 512, 2048);
        SB();
      }
      float s0 = c0[0], s1 = c1[0], mx = 0.f;
#pragma unroll
      for (int i = 0; i < 16; ++i) {
        o[i & 3] = MFMA32(vf[i], __builtin_bit_cast(bf16x8, pw[i >> 2]), o[i & 3]);
        if (i < 12) vf[i + 4] = tr2(vbp + ((i + 4) >> 2) * 4096 + (i & 3) * 512, 2048);
        if (i > 0) {
          asm volatile("v_add_f32_e32 %0, %0, %1" : "+v"(s0) : "v"(c0[i]));
          asm volatile("v_add_f32_e32 %0, %0, %1" : "+v"(s1) : "v"(c1[i]));
        }
        if (doqk) {
          if (i == 1) mx = fmaxf(n0[0], n1[0]);
          if (i >= 1) mx = fmaxf(fmaxf(mx, n0[i]), n1[i]);
        }
        if ((i & 3) == 1) dma(i >> 2);
        SB();
      }
      ADV(wsl);
      l += s0 + s1;
      if (doqk) {
        if (__any(mx > THR)) {
          float rm = fmaxf(mx, swap32(mx));
          float delta = fmaxf(rm, 0.f);
          float alpha = __builtin_amdgcn_exp2f(-delta);
          m += delta;
          l *= alpha;
#pragma unroll
          for (int r = 0; r < 16; ++r) {
            n0[r] -= delta;
            n1[r] -= delta;
            negm[r] = -m;
          }
#pragma unroll
          for (int cc = 0; cc < 4; ++cc)
#pragma unroll
            for (int r = 0; r < 16; ++r) o[cc][r] *= alpha;
        }
      }
    };
    WAITBAR(12);
    wsl = wsl == 0 ? 4 : wsl - 1;
    astep(A0, A1, B0, B1, T_{}, [&](int) {});
    for (int j = 1; j < 11; j += 2) {
      WAITBAR(8);
      astep(B0, B1, A0, A1, T_{}, [&](int pc) { piece_kv(Kh, Vh, j + 4, wsl, pc); });
      WAITBAR(8);
      astep(A0, A1, B0, B1, T_{}, [&](int pc) { piece_kv(Kh, Vh, j + 5, wsl, pc); });
    }
    WAITBAR(8);
    astep(B0, B1, A0, A1, T_{}, [&](int pc) { piece_kv(Kh, Vh, 15, wsl, pc); });
    WAITBAR(8);
    astep(A0, A1, B0, B1, T_{}, [&](int pc) { if (item == 0) piece_wx(nwq, nx, 0, wsl, pc); });
    WAITBAR(4);
    astep(B0, B1, A0, A1, T_{}, [&](int pc) { if (item == 0) piece_wx(nwq, nx, 1, wsl, pc); });
    WAITBAR(0);
    astep(A0, A1, B0, B1, T_{}, [&](int pc) { if (item == 0) piece_wx(nwq, nx, 2, wsl, pc); });
    WAITBAR(0);
    astep(B0, B1, A0, A1, F_{}, [&](int pc) { if (item == 0) piece_wx(nwq, nx, 3, wsl, pc); });
    l += swap32(l);
    const float inv = 1.0f / l;
    unsigned short* dst = ows + ((((size_t)(item * 16 + b) * 4 + tb) * 32 + (2 * hkv + g)) * 8) * 512 + lane * 8;
#pragma unroll
    for (int cc = 0; cc < 4; ++cc) {
      *reinterpret_cast<bf16x8*>(dst + (cc * 2 + 0) * 512) = pack8(o[cc], 0, inv);
      *reinterpret_cast<bf16x8*>(dst + (cc * 2 + 1) * 512) = pack8(o[cc], 8, inv);
    }
  }
}

constexpr int ST2 = 53248;
#define MFMA16(a, b, c) __builtin_amdgcn_mfma_f32_16x16x32_bf16(a, b, c, 0, 0, 0)
__global__ __launch_bounds__(512, 2) void oproj_kernel(gptr ows, gptr woA, gptr woB, float* __restrict__ part) {
  extern __shared__ __attribute__((aligned(16))) char shm[];
  const int tid = threadIdx.x, lane = tid & 63;
  const int w = __builtin_amdgcn_readfirstlane(tid >> 6);
  const int tg = w >> 1, ch = w & 1;
  const int ii = blockIdx.x, xcd = ii & 7, jj = ii >> 3;
  const int prob = xcd >> 2, kq = xcd & 3, tbk = jj >> 1, cH = jj & 1;
  gptr wog = (prob ? woB : woA) + (size_t)(kq * 1024) * 576 + cH * 288;
  gptr og = ows + ((size_t)(prob * 64 + tbk * 4 + tg) * 128 + kq * 32 + ch) * 1024;
  const unsigned ol = lane * 16;
  const int p0 = w < 4 ? 5 * w : 4 * w + 4;
  unsigned goff[5];
#pragma unroll
  for (int pc = 0; pc < 5; ++pc) {
    int L = min(p0 + pc, 35) * 64 + lane, row = L / 36, sg = L % 36;
    goff[pc] = row * 1152 + (sg ^ (((row >> 2) & 1) << 1)) * 16;
  }
  const unsigned l0 = ldsaddr(shm);
  auto piece = [&](int st, int slot, int pc) {
    unsigned d = l0 + slot * ST2;
    if (pc < 2) glds(ol, og + (size_t)st * 2048 + pc * 512, d + 36864 + tg * 4096 + ch * 2048 + pc * 1024);
    else if (pc < 6 || w < 4) glds(goff[pc - 2], wog + (size_t)st * (64 * 576), d + (p0 + pc - 2) * 1024);
  };
  const int gq = lane >> 4, g1 = gq & 1, q4 = (lane & 15) >> 2, p4 = lane & 3;
  const int abase = (16 * (gq >> 1) + 4 * g1 + q4) * 576 + ch * 288 + p4 * 8;
  const int a0 = abase + (ch ? -32 : 32) * g1, a1 = abase + (ch ? 32 : -32) * g1;
  const int ob = 36864 + tg * 4096 + (gq >> 1) * 1024 + (g1 * 32 + (lane & 15)) * 16;
  f32x4 acc[2][9] = {};
  bf16x8 fa[11], fb[11];
  auto step = [&](bf16x8* Fn, bf16x8* Fc, int slot, int hs, auto domm, auto dma) {
    const char* s = shm + slot * ST2;
#pragma unroll
    for (int cb = 0; cb < 9; ++cb) {
      if (domm) {
        acc[0][cb] = MFMA16(Fc[cb], Fc[9], acc[0][cb]);
        acc[1][cb] = MFMA16(Fc[cb], Fc[10], acc[1][cb]);
      }
      if (cb == 0) {
        Fn[9] = ld128(s + ob + hs * 2048);
        Fn[10] = ld128(s + ob + hs * 2048 + 256);
      }
      Fn[cb] = tr2(s + ((cb & 1) ? a1 : a0) + hs * 18432 + cb * 32, 4608);
      if (cb & 1) dma(cb >> 1);
      SB();
    }
  };
  struct T_ { constexpr operator bool() const { return true; } };
  struct F_ { constexpr operator bool() const { return false; } };
#pragma unroll
  for (int pc = 0; pc < 7; ++pc) piece(0, 0, pc);
#pragma unroll
  for (int pc = 0; pc < 7; ++pc) piece(1, 1, pc);
  int rs = 0, wsl = 2;
  if (w < 4) WAITBAR(7); else WAITBAR(6);
  step(fa, fb, rs, 0, F_{}, [&](int pc) { piece(2, wsl, pc); });
  step(fb, fa, rs, 1, T_{}, [&](int pc) { if (pc < 3) piece(2, wsl, 4 + pc); });
  rs = 1; wsl = 0;
  for (int S = 1; S < 15; ++S) {
    if (w < 4) WAITBAR(7); else WAITBAR(6);
    step(fa, fb, rs, 0, T_{}, [&](int pc) { if (S < 14) piece(S + 2, wsl, pc); });
    step(fb, fa, rs, 1, T_{}, [&](int pc) { if (S < 14 && pc < 3) piece(S + 2, wsl, 4 + pc); });
    rs = rs == 2 ? 0 : rs + 1;
    wsl = wsl == 2 ? 0 : wsl + 1;
  }
  WAITBAR(0);
  step(fa, fb, rs, 0, T_{}, [&](int) {});
  step(fb, fa, rs, 1, T_{}, [&](int) {});
#pragma unroll
  for (int cb = 0; cb < 9; ++cb) {
    acc[0][cb] = MFMA16(fb[cb], fb[9], acc[0][cb]);
    acc[1][cb] = MFMA16(fb[cb], fb[10], acc[1][cb]);
  }
  f32x4* dst = reinterpret_cast<f32x4*>(part) + ((size_t)(((kq * 2 + prob) * 16 + tbk) * 2 + cH) * 8 + w) * (18 * 64) + lane;
#pragma unroll
  for (int u = 0; u < 2; ++u)
#pragma unroll
    for (int cb = 0; cb < 9; ++cb) dst[(u * 9 + cb) * 64] = acc[u][cb];
}

__global__ __launch_bounds__(256) void reduce_kernel(const float* __restrict__ part, unsigned short* __restrict__ out) {
  const int gid = blockIdx.x * 256 + threadIdx.x, lane = gid & 63, wv = gid >> 6;
  const int blk = wv % 18, tw = wv / 18, w = tw & 7, tile = tw >> 3;
  const int cH = tile & 1, tbk = (tile >> 1) & 15, prob = tile >> 5;
  const int u = blk / 9, cb = blk % 9;
  const f32x4* p = reinterpret_cast<const f32x4*>(part) + gid;
  constexpr size_t KQ = (size_t)64 * 8 * 18 * 64;
  f32x4 s0 = p[0], s1 = p[KQ], s2 = p[2 * KQ], s3 = p[3 * KQ];
  f32x4 s = (s0 + s1) + (s2 + s3);
  const int tok = tbk * 128 + (w >> 1) * 32 + u * 16 + (lane & 15);
  const int col = prob * 576 + cH * 288 + (w & 1) * 144 + cb * 16 + 4 * (lane >> 4);
  u32x2 r = {cvtpk(s[0], s[1]), cvtpk(s[2], s[3])};
  *reinterpret_cast<u32x2*>(out + (size_t)tok * 1152 + col) = r;
}

extern "C" void kernel_launch(void* const* d_in, const int* in_sizes, int n_in, void* d_out, int out_size, void* d_ws,
                              size_t ws_size, hipStream_t stream) {
  (void)in_sizes; (void)n_in; (void)out_size; (void)ws_size;
  gptr xA = (gptr)d_in[0], kA = (gptr)d_in[1], vA = (gptr)d_in[2], wqA = (gptr)d_in[3], woA = (gptr)d_in[4];
  gptr xB = (gptr)d_in[5], kB = (gptr)d_in[6], vB = (gptr)d_in[7], wqB = (gptr)d_in[8], woB = (gptr)d_in[9];
  unsigned short* ows = (unsigned short*)d_ws;
  float* part = (float*)((char*)d_ws + ((size_t)1 << 25));
  hipFuncSetAttribute((const void*)attn_kernel, hipFuncAttributeMaxDynamicSharedMemorySize, 163840);
  hipFuncSetAttribute((const void*)oproj_kernel, hipFuncAttributeMaxDynamicSharedMemorySize, 163840);
  hipLaunchKernelGGL(attn_kernel, dim3(256), dim3(512), 163840, stream, xA, kA, vA, wqA, xB, kB, vB, wqB, ows);
  hipLaunchKernelGGL(oproj_kernel, dim3(256), dim3(512), 3 * ST2, stream, (gptr)ows, woA, woB, part);
  hipLaunchKernelGGL(reduce_kernel, dim3(2304), dim3(256), 0, stream, (const float*)part, (unsigned short*)d_out);
}
```

```cpp
#include <hip/hip_runtime.h>
#include <hip/hip_bf16.h>
#include <stdint.h>

typedef __attribute__((ext_vector_type(8))) short bf16x8;
typedef __attribute__((ext_vector_type(4))) short s16x4;
typedef __attribute__((ext_vector_type(16))) float f32x16;
typedef __attribute__((ext_vector_type(4))) float f32x4;
typedef __attribute__((ext_vector_type(2))) float f32x2;
typedef __attribute__((ext_vector_type(2))) __bf16 bf16x2;
typedef __attribute__((ext_vector_type(4))) unsigned u32x4;
typedef __attribute__((ext_vector_type(2))) unsigned u32x2;
typedef __attribute__((address_space(3))) s16x4* lds_s16x4_p;
typedef __attribute__((address_space(3))) void* lds_void_p;
typedef const unsigned short* gptr;

#define DEV __device__ __forceinline__

DEV unsigned cvtpk(float lo, float hi) {
  f32x2 v = {lo, hi};
  bf16x2 r = __builtin_convertvector(v, bf16x2);
  return __builtin_bit_cast(unsigned, r);
}
DEV bf16x8 pack8(const f32x16& a, int base, float sc) {
  u32x4 w = {cvtpk(a[base + 0] * sc, a[base + 1] * sc), cvtpk(a[base + 2] * sc, a[base + 3] * sc),
             cvtpk(a[base + 4] * sc, a[base + 5] * sc), cvtpk(a[base + 6] * sc, a[base + 7] * sc)};
  return __builtin_bit_cast(bf16x8, w);
}
DEV bf16x8 pack8n(const f32x16& a, int base) {
  u32x4 w = {cvtpk(a[base + 0], a[base + 1]), cvtpk(a[base + 2], a[base + 3]),
             cvtpk(a[base + 4], a[base + 5]), cvtpk(a[base + 6], a[base + 7])};
  return __builtin_bit_cast(bf16x8, w);
}
DEV void glds(unsigned voff, const void* sbase, unsigned lds) {
  unsigned keep;
  asm volatile("s_mov_b32 %0, m0\n\ts_mov_b32 m0, %3\n\ts_nop 0\n\tglobal_load_lds_dwordx4 %1, %2\n\ts_mov_b32 m0, %0"
               : "=&s"(keep) : "v"(voff), "s"(sbase), "s"(lds) : "memory");
}
DEV unsigned ldsaddr(const char* p) { return (unsigned)(size_t)(lds_void_p)p; }
#define SB() __builtin_amdgcn_sched_barrier(0)
DEV s16x4 trrd(const char* l) { return __builtin_amdgcn_ds_read_tr16_b64_v4i16((lds_s16x4_p)l); }
DEV bf16x8 tr2(const char* l, int d) {
  s16x4 a = trrd(l), b = trrd(l + d);
  return __builtin_shufflevector(a, b, 0, 1, 2, 3, 4, 5, 6, 7);
}
DEV bf16x8 ld128(const char* l) { return *reinterpret_cast<const bf16x8*>(l); }
DEV float swap32(float x) {
  auto rr = __builtin_amdgcn_permlane32_swap(__float_as_uint(x), __float_as_uint(x), false, false);
  return __uint_as_float((threadIdx.x & 32) ? rr[0] : rr[1]);
}
#define WAITBAR(n) asm volatile("s_waitcnt vmcnt(" #n ") lgkmcnt(0)\n\ts_barrier" ::: "memory")

constexpr int SLOT = 32768;
constexpr float QSCALE = 0.088388347648318447f * 1.4426950408889634f;
constexpr float THR = 8.0f;

#define MFMA32(a, b, c) __builtin_amdgcn_mfma_f32_32x32x16_bf16(a, b, c, 0, 0, 0)
__global__ __launch_bounds__(512, 2) void attn_kernel(gptr xA, gptr kA, gptr vA, gptr wqA, gptr xB, gptr kB, gptr vB,
                                                      gptr wqB, unsigned short* __restrict__ ows, unsigned* __restrict__ cnt) {
  extern __shared__ __attribute__((aligned(16))) char shm[];
  const int tid = threadIdx.x, lane = tid & 63;
  const int w = __builtin_amdgcn_readfirstlane(tid >> 6);
  const int ii = blockIdx.x, xcd = ii & 7, jj = ii >> 3;
  const int hkv = 2 * xcd + (jj & 1), b = jj >> 1;
  const int g = w >> 2, tb = w & 3;
  const int h = lane >> 5, l31 = lane & 31;
  if (ii < 2) cnt[ii * 512 + tid] = 0;
  const size_t kvoff = (size_t)(b * 16 + hkv) * (1024 * 128);

  const int sub = lane >> 5, row7 = (lane & 31) >> 2, slot4 = lane & 3;
  const unsigned kg = 2 * ((8 * w + row7) * 128 + sub * 32 + ((slot4 ^ ((2 * w + (row7 >> 2)) & 3)) * 8));
  const unsigned vg = 2 * ((8 * w + row7) * 128 + sub * 32 + slot4 * 8);
  const unsigned wg = 2 * ((8 * (w >> 1) + row7) * 4096 + ((2 * w) & 3) * 64 + sub * 32 + slot4 * 8);
  const unsigned xg = 2 * ((16 * w + (lane >> 2)) * 576 + ((slot4 ^ ((lane >> 4) & 3)) * 8));
  const unsigned l0 = ldsaddr(shm);

  auto piece_kv = [&](gptr Kh, gptr Vh, int t, int slot, int pc) {
    unsigned dst = l0 + slot * SLOT + w * 2048;
    if (pc == 0) glds(kg, Kh + t * 8192, dst);
    if (pc == 1) glds(kg, Kh + t * 8192 + 64, dst + 1024);
    if (pc == 2) glds(vg, Vh + t * 8192, dst + 16384);
    if (pc == 3) glds(vg, Vh + t * 8192 + 64, dst + 16384 + 1024);
  };
  auto piece_wx = [&](gptr wqh, gptr xbp, int c, int slot, int pc) {
    unsigned base = l0 + slot * SLOT;
    gptr ws_ = wqh + (size_t)c * (32 * 4096);
    if (pc == 0) glds(wg, ws_, base + w * 2048);
    if (pc == 1) glds(wg, ws_ + 64, base + w * 2048 + 1024);
    if (pc == 2) glds(xg, xbp + c * 32, base + 16384 + w * 1024);
  };
  auto issue_wx = [&](gptr wqh, gptr xbp, int c, int slot) {
    piece_wx(wqh, xbp, c, slot, 0);
    piece_wx(wqh, xbp, c, slot, 1);
    piece_wx(wqh, xbp, c, slot, 2);
  };

  const int q4 = (lane & 15) >> 2, p4 = lane & 3, blk = (lane >> 4) & 1;
  const int p4s = ((p4 & 1) << 1) | (p4 >> 1);
  const int wbase = h * 4096 + g * 2048 + q4 * 64 + blk * 32 + p4s * 8;
  const int xrow = 32 * tb + l31;
  const int xb0 = 16384 + 512 * (xrow >> 3) + 64 * (xrow & 7) + 16 * ((0 + h) ^ ((xrow >> 2) & 3));
  const int xb1 = 16384 + 512 * (xrow >> 3) + 64 * (xrow & 7) + 16 * ((2 + h) ^ ((xrow >> 2) & 3));
  const int kb0 = 2048 * (l31 >> 3) + 64 * (l31 & 7) + 16 * ((0 + h) ^ ((l31 >> 2) & 3));
  const int kb1 = 2048 * (l31 >> 3) + 64 * (l31 & 7) + 16 * ((2 + h) ^ ((l31 >> 2) & 3));
  const int vb = 16384 + h * 256 + q4 * 64 + blk * 32 + p4 * 8;

  int rs = 0, wsl = 4;
#define ADV(x) x = (x == 4 ? 0 : x + 1)
  struct T_ { constexpr operator bool() const { return true; } };
  struct F_ { constexpr operator bool() const { return false; } };
  {
    gptr wqh = wqA + hkv * 256, xbp = xA + (size_t)b * (128 * 576);
    issue_wx(wqh, xbp, 0, 0);
    issue_wx(wqh, xbp, 1, 1);
    issue_wx(wqh, xbp, 2, 2);
    issue_wx(wqh, xbp, 3, 3);
  }
  gptr nwq = wqB + hkv * 256, nx = xB + (size_t)b * (128 * 576);
  for (int item = 0; item < 2; ++item) {
    gptr wqh = (item ? wqB : wqA) + hkv * 256, xbp = (item ? xB : xA) + (size_t)b * (128 * 576);
    gptr Kh = (item ? kB : kA) + kvoff, Vh = (item ? vB : vA) + kvoff;
    f32x16 acc[4] = {};
    bf16x8 fa[10], fb[10];
    auto qstep = [&](bf16x8* Fn, bf16x8* Fc, auto domm, auto dma) {
      const char* base = shm + rs * SLOT;
      ADV(rs);
#pragma unroll
      for (int i = 0; i < 8; ++i) {
        if (domm) acc[i & 3] = MFMA32(Fc[i], Fc[8 + (i >> 2)], acc[i & 3]);
        if (i == 0) {
          Fn[8] = ld128(base + xb0);
          Fn[9] = ld128(base + xb1);
        }
        Fn[i] = tr2(base + wbase + (i >> 2) * 8192 + (i & 3) * 512, 256);
        if (i & 1) dma(i >> 1);
        SB();
      }
      ADV(wsl);
    };
    WAITBAR(9);
    qstep(fa, fb, F_{}, [&](int pc) { piece_wx(wqh, xbp, 4, wsl, pc); });
    for (int c = 0; c < 12; c += 2) {
      WAITBAR(9);
      qstep(fb, fa, T_{}, [&](int pc) { piece_wx(wqh, xbp, c + 5, wsl, pc); });
      WAITBAR(9);
      qstep(fa, fb, T_{}, [&](int pc) { piece_wx(wqh, xbp, c + 6, wsl, pc); });
    }
    WAITBAR(9);
    qstep(fb, fa, T_{}, [&](int pc) { piece_wx(wqh, xbp, 17, wsl, pc); });
    for (int c = 0; c < 4; c += 2) {
      WAITBAR(9);
      qstep(fa, fb, T_{}, [&](int pc) { piece_kv(Kh, Vh, c, wsl, pc); });
      WAITBAR(9);
      qstep(fb, fa, T_{}, [&](int pc) { piece_kv(Kh, Vh, c + 1, wsl, pc); });
    }
    WAITBAR(9);
#pragma unroll
    for (int i = 0; i < 8; ++i) {
      acc[i & 3] = MFMA32(fb[i], fb[8 + (i >> 2)], acc[i & 3]);
      if (i & 1) piece_kv(Kh, Vh, 4, wsl, i >> 1);
      SB();
    }
    ADV(wsl);
    bf16x8 qf[4][2];
#pragma unroll
    for (int cc = 0; cc < 4; ++cc) {
      qf[cc][0] = pack8(acc[cc], 0, QSCALE);
      qf[cc][1] = pack8(acc[cc], 8, QSCALE);
    }

    f32x16 o[4] = {};
    float m, l = 0.f;
    f32x16 negm = {};
    f32x16 A0, A1, B0, B1;
    {
      const char* kp0 = shm + rs * SLOT + kb0;
      const char* kp1 = shm + rs * SLOT + kb1;
      bf16x8 kf[16];
#pragma unroll
      for (int i = 0; i < 16; ++i) kf[i] = ld128(((i & 2) ? kp1 : kp0) + (i & 1) * 8192 + (i >> 2) * 512);
#pragma unroll
      for (int i = 0; i < 16; ++i) {
        if (i & 1) A1 = MFMA32(kf[i], qf[i >> 2][(i >> 1) & 1], i < 2 ? negm : A1);
        else A0 = MFMA32(kf[i], qf[i >> 2][(i >> 1) & 1], i < 2 ? negm : A0);
      }
      float mx = fmaxf(A0[0], A1[0]);
#pragma unroll
      for (int r = 1; r < 16; ++r) mx = fmaxf(fmaxf(mx, A0[r]), A1[r]);
      m = fmaxf(mx, swap32(mx));
#pragma unroll
      for (int r = 0; r < 16; ++r) {
        A0[r] -= m;
        A1[r] -= m;
        negm[r] = -m;
      }
      SB();
    }
    auto astep = [&](f32x16& c0, f32x16& c1, f32x16& n0, f32x16& n1, auto doqk, auto dma) {
      const char* vbp = shm + rs * SLOT + vb;
      ADV(rs);
      const char* kp0 = shm + rs * SLOT + kb0;
      const char* kp1 = shm + rs * SLOT + kb1;
      bf16x8 kf[16], vf[16];
      u32x4 pw[4];
      if (doqk) {
#pragma unroll
        for (int i = 0; i < 4; ++i) kf[i] = ld128(((i & 2) ? kp1 : kp0) + (i & 1) * 8192 + (i >> 2) * 512);
      }
#pragma unroll
      for (int r = 0; r < 4; ++r) c0[r] = __builtin_amdgcn_exp2f(c0[r]);
      SB();
#pragma unroll
      for (int i = 0; i < 16; ++i) {
        if (doqk) {
          if (i & 1) n1 = MFMA32(kf[i], qf[i >> 2][(i >> 1) & 1], i < 2 ? negm : n1);
          else n0 = MFMA32(kf[i], qf[i >> 2][(i >> 1) & 1], i < 2 ? negm : n0);
          if (i < 12) kf[i + 4] = ld128((((i + 4) & 2) ? kp1 : kp0) + (i & 1) * 8192 + ((i + 4) >> 2) * 512);
        }
        if (i < 6) {
          c0[4 + 2 * i] = __builtin_amdgcn_exp2f(c0[4 + 2 * i]);
          c0[5 + 2 * i] = __builtin_amdgcn_exp2f(c0[5 + 2 * i]);
        } else if (i < 14) {
          c1[2 * i - 12] = __builtin_amdgcn_exp2f(c1[2 * i - 12]);
          c1[2 * i - 11] = __builtin_amdgcn_exp2f(c1[2 * i - 11]);
        }
        pw[i >> 2][i & 3] = i < 8 ? cvtpk(c0[2 * i], c0[2 * i + 1]) : cvtpk(c1[2 * i - 16], c1[2 * i - 15]);
        if (i >= 12) vf[i - 12] = tr2(vbp + (i - 12) * 512, 2048);
        SB();
      }
      float s0 = c0[0], s1 = c1[0], mx = 0.f;
#pragma unroll
      for (int i = 0; i < 16; ++i) {
        o[i & 3] = MFMA32(vf[i], __builtin_bit_cast(bf16x8, pw[i >> 2]), o[i & 3]);
        if (i < 12) vf[i + 4] = tr2(vbp + ((i + 4) >> 2) * 4096 + (i & 3) * 512, 2048);
        if (i > 0) {
          asm volatile("v_add_f32_e32 %0, %0, %1" : "+v"(s0) : "v"(c0[i]));
          asm volatile("v_add_f32_e32 %0, %0, %1" : "+v"(s1) : "v"(c1[i]));
        }
        if (doqk) {
          if (i == 1) mx = fmaxf(n0[0], n1[0]);
          if (i >= 1) mx = fmaxf(fmaxf(mx, n0[i]), n1[i]);
        }
        if ((i & 3) == 1) dma(i >> 2);
        SB();
      }
      ADV(wsl);
      l += s0 + s1;
      if (doqk) {
        if (__any(mx > THR)) {
          float rm = fmaxf(mx, swap32(mx));
          float delta = fmaxf(rm, 0.f);
          float alpha = __builtin_amdgcn_exp2f(-delta);
          m += delta;
          l *= alpha;
#pragma unroll
          for (int r = 0; r < 16; ++r) {
            n0[r] -= delta;
            n1[r] -= delta;
            negm[r] = -m;
          }
#pragma unroll
          for (int cc = 0; cc < 4; ++cc)
#pragma unroll
            for (int r = 0; r < 16; ++r) o[cc][r] *= alpha;
        }
      }
    };
    WAITBAR(12);
    wsl = wsl == 0 ? 4 : wsl - 1;
    astep(A0, A1, B0, B1, T_{}, [&](int) {});
    for (int j = 1; j < 11; j += 2) {
      WAITBAR(8);
      astep(B0, B1, A0, A1, T_{}, [&](int pc) { piece_kv(Kh, Vh, j + 4, wsl, pc); });
      WAITBAR(8);
      astep(A0, A1, B0, B1, T_{}, [&](int pc) { piece_kv(Kh, Vh, j + 5, wsl, pc); });
    }
    WAITBAR(8);
    astep(B0, B1, A0, A1, T_{}, [&](int pc) { piece_kv(Kh, Vh, 15, wsl, pc); });
    WAITBAR(8);
    astep(A0, A1, B0, B1, T_{}, [&](int pc) { if (item == 0) piece_wx(nwq, nx, 0, wsl, pc); });
    WAITBAR(4);
    astep(B0, B1, A0, A1, T_{}, [&](int pc) { if (item == 0) piece_wx(nwq, nx, 1, wsl, pc); });
    WAITBAR(0);
    astep(A0, A1, B0, B1, T_{}, [&](int pc) { if (item == 0) piece_wx(nwq, nx, 2, wsl, pc); });
    WAITBAR(0);
    astep(B0, B1, A0, A1, F_{}, [&](int pc) { if (item == 0) piece_wx(nwq, nx, 3, wsl, pc); });
    l += swap32(l);
    const float inv = 1.0f / l;
    unsigned short* dst = ows + ((((size_t)(item * 16 + b) * 4 + tb) * 32 + (2 * hkv + g)) * 8) * 512 + lane * 8;
#pragma unroll
    for (int cc = 0; cc < 4; ++cc) {
      *reinterpret_cast<bf16x8*>(dst + (cc * 2 + 0) * 512) = pack8(o[cc], 0, inv);
      *reinterpret_cast<bf16x8*>(dst + (cc * 2 + 1) * 512) = pack8(o[cc], 8, inv);
    }
  }
}

constexpr int ST2 = 26624;
#define MFMA16(a, b, c) __builtin_amdgcn_mfma_f32_16x16x32_bf16(a, b, c, 0, 0, 0)
__global__ __launch_bounds__(512, 2) void oproj_kernel(gptr ows, gptr woA, gptr woB, float* part, unsigned* cnt,
                                                       unsigned short* __restrict__ out) {
  extern __shared__ __attribute__((aligned(16))) char shm[];
  const int tid = threadIdx.x, lane = tid & 63;
  const int w = __builtin_amdgcn_readfirstlane(tid >> 6);
  const int tg = w >> 1, ch = w & 1;
  const int ii = blockIdx.x, xcd = ii & 7, jj = ii >> 3;
  const int prob = xcd >> 2, kq = xcd & 3, tbk = jj >> 1, cH = jj & 1;
  gptr wog = (prob ? woB : woA) + (size_t)(kq * 1024) * 576 + cH * 288;
  gptr og = ows + ((size_t)(prob * 64 + tbk * 4 + tg) * 128 + kq * 32) * 1024 + ch * 512;
  const unsigned ol = lane * 16;
  unsigned goff[3];
#pragma unroll
  for (int pc = 0; pc < 3; ++pc) {
    int L = (pc < 2 ? 2 * w + pc : 16 + (w & 1)) * 64 + lane, row = L / 36, sg = L % 36;
    goff[pc] = row * 1152 + (sg ^ (((row >> 2) & 1) << 1)) * 16;
  }
  const unsigned l0 = ldsaddr(shm);
  auto piece = [&](int st, int slot, int pc) {
    unsigned d = l0 + slot * ST2;
    if (pc == 0) glds(ol, og + (size_t)st * 1024, d + 18432 + w * 1024);
    else if (pc < 3) glds(goff[pc - 1], wog + (size_t)st * (32 * 576), d + (2 * w + pc - 1) * 1024);
    else if (w < 2) glds(goff[2], wog + (size_t)st * (32 * 576), d + (16 + w) * 1024);
  };
  const int gq = lane >> 4, g1 = gq & 1, q4 = (lane & 15) >> 2, p4 = lane & 3;
  const int abase = (16 * (gq >> 1) + 4 * g1 + q4) * 576 + ch * 288 + p4 * 8;
  const int a0 = abase + (ch ? -32 : 32) * g1, a1 = abase + (ch ? 32 : -32) * g1;
  const int ob = 18432 + tg * 2048 + (gq >> 1) * 1024 + (g1 * 32 + (lane & 15)) * 16;
  f32x4 acc[2][9] = {};
  bf16x8 fa[11], fb[11];
  int rs = 0;
  auto step = [&](bf16x8* Fn, bf16x8* Fc, auto domm, auto dma) {
    const char* s = shm + rs * ST2;
#pragma unroll
    for (int cb = 0; cb < 9; ++cb) {
      if (domm) {
        acc[0][cb] = MFMA16(Fc[cb], Fc[9], acc[0][cb]);
        acc[1][cb] = MFMA16(Fc[cb], Fc[10], acc[1][cb]);
      }
      if (cb == 0) {
        Fn[9] = ld128(s + ob);
        Fn[10] = ld128(s + ob + 256);
      }
      Fn[cb] = tr2(s + ((cb & 1) ? a1 : a0) + cb * 32, 4608);
      if (cb & 1) dma(cb >> 1);
      SB();
    }
    rs = rs == 5 ? 0 : rs + 1;
  };
  struct T_ { constexpr operator bool() const { return true; } };
  struct F_ { constexpr operator bool() const { return false; } };
#define WB2(n4, n3) do { if (w < 2) WAITBAR(n4); else WAITBAR(n3); } while (0)
  for (int st = 0; st < 5; ++st)
#pragma unroll
    for (int pc = 0; pc < 4; ++pc) piece(st, st, pc);
  WB2(16, 12);
  step(fa, fb, F_{}, [&](int pc) { piece(5, 5, pc); });
  for (int n = 1; n < 27; n += 2) {
    WB2(16, 12);
    step(fb, fa, T_{}, [&](int pc) { piece(n + 5, rs == 0 ? 5 : rs - 1, pc); });
    WB2(16, 12);
    step(fa, fb, T_{}, [&](int pc) { piece(n + 6, rs == 0 ? 5 : rs - 1, pc); });
  }
  unsigned* cw = cnt + ((((prob * 16 + tbk) * 2 + cH) * 8 + w) << 1);
  unsigned ticket = 0;
  if (lane == 0) ticket = __hip_atomic_fetch_add(cw, 1u, __ATOMIC_RELAXED, __HIP_MEMORY_SCOPE_AGENT);
  WB2(16, 12);
  step(fb, fa, T_{}, [&](int) {});
  WB2(12, 9);
  step(fa, fb, T_{}, [&](int) {});
  WB2(8, 6);
  step(fb, fa, T_{}, [&](int) {});
  WB2(4, 3);
  step(fa, fb, T_{}, [&](int) {});
  WAITBAR(0);
  step(fb, fa, T_{}, [&](int) {});
#pragma unroll
  for (int cb = 0; cb < 9; ++cb) {
    acc[0][cb] = MFMA16(fb[cb], fb[9], acc[0][cb]);
    acc[1][cb] = MFMA16(fb[cb], fb[10], acc[1][cb]);
  }
  ticket = __builtin_amdgcn_readfirstlane(ticket);
  const unsigned pofs = (unsigned)(((((prob * 16 + tbk) * 2 + cH) * 8 + w) * (18 * 64) + lane) * 16);
  constexpr unsigned KQB = 64u * 8 * 18 * 64 * 16;
  if (ticket < 3) {
    __amdgpu_buffer_rsrc_t rsrc = __builtin_amdgcn_make_buffer_rsrc((void*)part, 0, 4 * KQB, 0x00020000);
#pragma unroll
    for (int u = 0; u < 2; ++u)
#pragma unroll
      for (int cb = 0; cb < 9; ++cb)
        __builtin_amdgcn_raw_buffer_store_b128(__builtin_bit_cast(u32x4, acc[u][cb]), rsrc,
                                               pofs + kq * KQB + (u * 9 + cb) * 1024, 0,   16);
    asm volatile("s_waitcnt vmcnt(0)" ::: "memory");
    if (lane == 0) __hip_atomic_fetch_add(cw + 1, 1u, __ATOMIC_RELAXED, __HIP_MEMORY_SCOPE_AGENT);
  } else {
    for (int spin = 0; spin < (1 << 24); ++spin) {
      unsigned d = __hip_atomic_load(cw + 1, __ATOMIC_RELAXED, __HIP_MEMORY_SCOPE_AGENT);
      if (__builtin_amdgcn_readfirstlane(d) >= 3) break;
      __builtin_amdgcn_s_sleep(2);
    }
    __builtin_amdgcn_fence(__ATOMIC_ACQUIRE, "agent");
    const char* pb = reinterpret_cast<const char*>(part) + pofs;
    const char* q1 = pb + (size_t)(kq ^ 1) * KQB;
    const char* q2 = pb + (size_t)(kq ^ 2) * KQB;
    const char* q3 = pb + (size_t)(kq ^ 3) * KQB;
    unsigned short* dst = out + (size_t)(tbk * 128 + tg * 32 + (lane & 15)) * 1152 + prob * 576 + cH * 288 + ch * 144 +
                          4 * gq;
#pragma unroll
    for (int u = 0; u < 2; ++u) {
      f32x4 v1[9], v2[9], v3[9];
#pragma unroll
      for (int cb = 0; cb < 9; ++cb) {
        v1[cb] = *reinterpret_cast<const f32x4*>(q1 + (u * 9 + cb) * 1024);
        v2[cb] = *reinterpret_cast<const f32x4*>(q2 + (u * 9 + cb) * 1024);
        v3[cb] = *reinterpret_cast<const f32x4*>(q3 + (u * 9 + cb) * 1024);
      }
#pragma unroll
      for (int cb = 0; cb < 9; ++cb) {
        f32x4 t = (acc[u][cb] + v1[cb]) + (v2[cb] + v3[cb]);
        u32x2 r = {cvtpk(t[0], t[1]), cvtpk(t[2], t[3])};
        *reinterpret_cast<u32x2*>(dst + u * (16 * 1152) + cb * 16) = r;
      }
    }
  }
}

extern "C" void kernel_launch(void* const* d_in, const int* in_sizes, int n_in, void* d_out, int out_size, void* d_ws,
                              size_t ws_size, hipStream_t stream) {
  (void)in_sizes; (void)n_in; (void)out_size; (void)ws_size;
  gptr xA = (gptr)d_in[0], kA = (gptr)d_in[1], vA = (gptr)d_in[2], wqA = (gptr)d_in[3], woA = (gptr)d_in[4];
  gptr xB = (gptr)d_in[5], kB = (gptr)d_in[6], vB = (gptr)d_in[7], wqB = (gptr)d_in[8], woB = (gptr)d_in[9];
  unsigned short* ows = (unsigned short*)d_ws;
  float* part = (float*)((char*)d_ws + ((size_t)1 << 25));
  hipFuncSetAttribute((const void*)attn_kernel, hipFuncAttributeMaxDynamicSharedMemorySize, 163840);
  hipFuncSetAttribute((const void*)oproj_kernel, hipFuncAttributeMaxDynamicSharedMemorySize, 163840);
  unsigned* cnt = (unsigned*)((char*)d_ws + ((size_t)72 << 20));
  hipLaunchKernelGGL(attn_kernel, dim3(256), dim3(512), 163840, stream, xA, kA, vA, wqA, xB, kB, vB, wqB, ows, cnt);
  hipLaunchKernelGGL(oproj_kernel, dim3(256), dim3(512), 6 * ST2, stream, (gptr)ows, woA, woB, part, cnt,
                     (unsigned short*)d_out);
}
```

```cpp
#include <hip/hip_runtime.h>
#include <hip/hip_bf16.h>
#include <stdint.h>

typedef __attribute__((ext_vector_type(8))) short bf16x8;
typedef __attribute__((ext_vector_type(4))) short s16x4;
typedef __attribute__((ext_vector_type(16))) float f32x16;
typedef __attribute__((ext_vector_type(4))) float f32x4;
typedef __attribute__((ext_vector_type(2))) float f32x2;
typedef __attribute__((ext_vector_type(2))) __bf16 bf16x2;
typedef __attribute__((ext_vector_type(4))) unsigned u32x4;
typedef __attribute__((ext_vector_type(2))) unsigned u32x2;
typedef __attribute__((address_space(3))) s16x4* lds_s16x4_p;
typedef __attribute__((address_space(3))) void* lds_void_p;
typedef const unsigned short* gptr;

#define DEV __device__ __forceinline__

DEV unsigned cvtpk(float lo, float hi) {
  f32x2 v = {lo, hi};
  bf16x2 r = __builtin_convertvector(v, bf16x2);
  return __builtin_bit_cast(unsigned, r);
}
DEV bf16x8 pack8(const f32x16& a, int base, float sc) {
  u32x4 w = {cvtpk(a[base + 0] * sc, a[base + 1] * sc), cvtpk(a[base + 2] * sc, a[base + 3] * sc),
             cvtpk(a[base + 4] * sc, a[base + 5] * sc), cvtpk(a[base + 6] * sc, a[base + 7] * sc)};
  return __builtin_bit_cast(bf16x8, w);
}
DEV bf16x8 pack8n(const f32x16& a, int base) {
  u32x4 w = {cvtpk(a[base + 0], a[base + 1]), cvtpk(a[base + 2], a[base + 3]),
             cvtpk(a[base + 4], a[base + 5]), cvtpk(a[base + 6], a[base + 7])};
  return __builtin_bit_cast(bf16x8, w);
}
DEV void glds(unsigned voff, const void* sbase, unsigned lds) {
  unsigned keep;
  asm volatile("s_mov_b32 %0, m0\n\ts_mov_b32 m0, %3\n\ts_nop 0\n\tglobal_load_lds_dwordx4 %1, %2\n\ts_mov_b32 m0, %0"
               : "=&s"(keep) : "v"(voff), "s"(sbase), "s"(lds) : "memory");
}
DEV void glds_nt(unsigned voff, const void* sbase, unsigned lds) {
  unsigned keep;
  asm volatile("s_mov_b32 %0, m0\n\ts_mov_b32 m0, %3\n\ts_nop 0\n\tglobal_load_lds_dwordx4 %1, %2 nt\n\ts_mov_b32 m0, %0"
               : "=&s"(keep) : "v"(voff), "s"(sbase), "s"(lds) : "memory");
}
DEV unsigned ldsaddr(const char* p) { return (unsigned)(size_t)(lds_void_p)p; }
#define SB() __builtin_amdgcn_sched_barrier(0)
DEV s16x4 trrd(const char* l) { return __builtin_amdgcn_ds_read_tr16_b64_v4i16((lds_s16x4_p)l); }
DEV bf16x8 tr2(const char* l, int d) {
  s16x4 a = trrd(l), b = trrd(l + d);
  return __builtin_shufflevector(a, b, 0, 1, 2, 3, 4, 5, 6, 7);
}
DEV bf16x8 ld128(const char* l) { return *reinterpret_cast<const bf16x8*>(l); }
DEV float swap32(float x) {
  auto rr = __builtin_amdgcn_permlane32_swap(__float_as_uint(x), __float_as_uint(x), false, false);
  return __uint_as_float((threadIdx.x & 32) ? rr[0] : rr[1]);
}
#define WAITBAR(n)                                                          \
  do {                                                                      \
    __builtin_amdgcn_s_waitcnt(0xC07F);                      \
    asm volatile("s_waitcnt vmcnt(" #n ")\n\ts_barrier" ::: "memory");     \
  } while (0)

constexpr int SLOT = 32768;
constexpr float QSCALE = 0.088388347648318447f * 1.4426950408889634f;
constexpr float THR = 8.0f;

#define MFMA32(a, b, c) __builtin_amdgcn_mfma_f32_32x32x16_bf16(a, b, c, 0, 0, 0)
__global__ __launch_bounds__(512, 2) void attn_kernel(gptr xA, gptr kA, gptr vA, gptr wqA, gptr xB, gptr kB, gptr vB,
                                                      gptr wqB, unsigned short* __restrict__ ows) {
  extern __shared__ __attribute__((aligned(16))) char shm[];
  const int tid = threadIdx.x, lane = tid & 63;
  const int w = __builtin_amdgcn_readfirstlane(tid >> 6);
  const int ii = blockIdx.x, xcd = ii & 7, jj = ii >> 3;
  const int hkv = 2 * xcd + (jj & 1), b = jj >> 1;
  const int g = w >> 2, tb = w & 3;
  const int h = lane >> 5, l31 = lane & 31;
  const size_t kvoff = (size_t)(b * 16 + hkv) * (1024 * 128);

  const int sub = lane >> 5, row7 = (lane & 31) >> 2, slot4 = lane & 3;
  const unsigned kg = 2 * ((8 * w + row7) * 128 + sub * 32 + ((slot4 ^ ((2 * w + (row7 >> 2)) & 3)) * 8));
  const unsigned vg = 2 * ((8 * w + row7) * 128 + sub * 32 + slot4 * 8);
  const unsigned wg = 2 * ((8 * (w >> 1) + row7) * 4096 + ((2 * w) & 3) * 64 + sub * 32 + slot4 * 8);
  const unsigned xg = 2 * ((16 * w + (lane >> 2)) * 576 + ((slot4 ^ ((lane >> 4) & 3)) * 8));
  const unsigned l0 = ldsaddr(shm);

  auto piece_kv = [&](gptr Kh, gptr Vh, int t, int slot, int pc) {
    unsigned dst = l0 + slot * SLOT + w * 2048;
    if (pc == 0) glds_nt(kg, Kh + t * 8192, dst);
    if (pc == 1) glds_nt(kg, Kh + t * 8192 + 64, dst + 1024);
    if (pc == 2) glds_nt(vg, Vh + t * 8192, dst + 16384);
    if (pc == 3) glds_nt(vg, Vh + t * 8192 + 64, dst + 16384 + 1024);
  };
  auto piece_wx = [&](gptr wqh, gptr xbp, int c, int slot, int pc) {
    unsigned base = l0 + slot * SLOT;
    gptr ws_ = wqh + (size_t)c * (32 * 4096);
    if (pc == 0) glds(wg, ws_, base + w * 2048);
    if (pc == 1) glds(wg, ws_ + 64, base + w * 2048 + 1024);
    if (pc == 2) glds(xg, xbp + c * 32, base + 16384 + w * 1024);
  };
  auto issue_wx = [&](gptr wqh, gptr xbp, int c, int slot) {
    piece_wx(wqh, xbp, c, slot, 0);
    piece_wx(wqh, xbp, c, slot, 1);
    piece_wx(wqh, xbp, c, slot, 2);
  };

  const int q4 = (lane & 15) >> 2, p4 = lane & 3, blk = (lane >> 4) & 1;
  const int p4s = ((p4 & 1) << 1) | (p4 >> 1);
  const int wbase = h * 4096 + g * 2048 + q4 * 64 + blk * 32 + p4s * 8;
  const int xrow = 32 * tb + l31;
  const int xb0 = 16384 + 512 * (xrow >> 3) + 64 * (xrow & 7) + 16 * ((0 + h) ^ ((xrow >> 2) & 3));
  const int xb1 = 16384 + 512 * (xrow >> 3) + 64 * (xrow & 7) + 16 * ((2 + h) ^ ((xrow >> 2) & 3));
  const int kb0 = 2048 * (l31 >> 3) + 64 * (l31 & 7) + 16 * ((0 + h) ^ ((l31 >> 2) & 3));
  const int kb1 = 2048 * (l31 >> 3) + 64 * (l31 & 7) + 16 * ((2 + h) ^ ((l31 >> 2) & 3));
  const int vb = 16384 + h * 256 + q4 * 64 + blk * 32 + p4 * 8;

  int rs = 0, wsl = 4;
#define ADV(x) x = (x == 4 ? 0 : x + 1)
  struct T_ { constexpr operator bool() const { return true; } };
  struct F_ { constexpr operator bool() const { return false; } };
  {
    gptr wqh = wqA + hkv * 256, xbp = xA + (size_t)b * (128 * 576);
    issue_wx(wqh, xbp, 0, 0);
    issue_wx(wqh, xbp, 1, 1);
    issue_wx(wqh, xbp, 2, 2);
    issue_wx(wqh, xbp, 3, 3);
  }
  gptr nwq = wqB + hkv * 256, nx = xB + (size_t)b * (128 * 576);
  for (int item = 0; item < 2; ++item) {
    gptr wqh = (item ? wqB : wqA) + hkv * 256, xbp = (item ? xB : xA) + (size_t)b * (128 * 576);
    gptr Kh = (item ? kB : kA) + kvoff, Vh = (item ? vB : vA) + kvoff;
    f32x16 acc[4] = {};
    bf16x8 fa[10], fb[10];
    auto qstep = [&](bf16x8* Fn, bf16x8* Fc, auto domm, auto dma) {
      const char* base = shm + rs * SLOT;
      ADV(rs);
#pragma unroll
      for (int i = 0; i < 8; ++i) {
        if (domm) acc[i & 3] = MFMA32(Fc[i], Fc[8 + (i >> 2)], acc[i & 3]);
        if (i == 0) {
          Fn[8] = ld128(base + xb0);
          Fn[9] = ld128(base + xb1);
        }
        Fn[i] = tr2(base + wbase + (i >> 2) * 8192 + (i & 3) * 512, 256);
        if (i & 1) dma(i >> 1);
        SB();
      }
      ADV(wsl);
    };
    WAITBAR(9);
    qstep(fa, fb, F_{}, [&](int pc) { piece_wx(wqh, xbp, 4, wsl, pc); });
    for (int c = 0; c < 12; c += 2) {
      WAITBAR(9);
      qstep(fb, fa, T_{}, [&](int pc) { piece_wx(wqh, xbp, c + 5, wsl, pc); });
      WAITBAR(9);
      qstep(fa, fb, T_{}, [&](int pc) { piece_wx(wqh, xbp, c + 6, wsl, pc); });
    }
    WAITBAR(9);
    qstep(fb, fa, T_{}, [&](int pc) { piece_wx(wqh, xbp, 17, wsl, pc); });
    for (int c = 0; c < 4; c += 2) {
      WAITBAR(9);
      qstep(fa, fb, T_{}, [&](int pc) { piece_kv(Kh, Vh, c, wsl, pc); });
      WAITBAR(9);
      qstep(fb, fa, T_{}, [&](int pc) { piece_kv(Kh, Vh, c + 1, wsl, pc); });
    }
    WAITBAR(9);
#pragma unroll
    for (int i = 0; i < 8; ++i) {
      acc[i & 3] = MFMA32(fb[i], fb[8 + (i >> 2)], acc[i & 3]);
      if (i & 1) piece_kv(Kh, Vh, 4, wsl, i >> 1);
      SB();
    }
    ADV(wsl);
    bf16x8 qf[4][2];
#pragma unroll
    for (int cc = 0; cc < 4; ++cc) {
      qf[cc][0] = pack8(acc[cc], 0, QSCALE);
      qf[cc][1] = pack8(acc[cc], 8, QSCALE);
    }

    f32x16 o[4] = {};
    float m, l = 0.f;
    f32x16 negm = {};
    f32x16 A0, A1, B0, B1;
    {
      const char* kp0 = shm + rs * SLOT + kb0;
      const char* kp1 = shm + rs * SLOT + kb1;
      bf16x8 kf[16];
#pragma unroll
      for (int i = 0; i < 16; ++i) kf[i] = ld128(((i & 2) ? kp1 : kp0) + (i & 1) * 8192 + (i >> 2) * 512);
#pragma unroll
      for (int i = 0; i < 16; ++i) {
        if (i & 1) A1 = MFMA32(kf[i], qf[i >> 2][(i >> 1) & 1], i < 2 ? negm : A1);
        else A0 = MFMA32(kf[i], qf[i >> 2][(i >> 1) & 1], i < 2 ? negm : A0);
      }
      float mx = fmaxf(A0[0], A1[0]);
#pragma unroll
      for (int r = 1; r < 16; ++r) mx = fmaxf(fmaxf(mx, A0[r]), A1[r]);
      m = fmaxf(mx, swap32(mx));
#pragma unroll
      for (int r = 0; r < 16; ++r) {
        A0[r] -= m;
        A1[r] -= m;
        negm[r] = -m;
      }
      SB();
    }
    auto astep = [&](f32x16& c0, f32x16& c1, f32x16& n0, f32x16& n1, auto doqk, auto dma) {
      const char* vbp = shm + rs * SLOT + vb;
      ADV(rs);
      const char* kp0 = shm + rs * SLOT + kb0;
      const char* kp1 = shm + rs * SLOT + kb1;
      bf16x8 kf[16], vf[16];
      u32x4 pw[4];
      if (doqk) {
#pragma unroll
        for (int i = 0; i < 4; ++i) kf[i] = ld128(((i & 2) ? kp1 : kp0) + (i & 1) * 8192 + (i >> 2) * 512);
      }
#pragma unroll
      for (int r = 0; r < 4; ++r) c0[r] = __builtin_amdgcn_exp2f(c0[r]);
      SB();
#pragma unroll
      for (int i = 0; i < 16; ++i) {
        if (doqk) {
          if (i & 1) n1 = MFMA32(kf[i], qf[i >> 2][(i >> 1) & 1], i < 2 ? negm : n1);
          else n0 = MFMA32(kf[i], qf[i >> 2][(i >> 1) & 1], i < 2 ? negm : n0);
          if (i < 12) kf[i + 4] = ld128((((i + 4) & 2) ? kp1 : kp0) + (i & 1) * 8192 + ((i + 4) >> 2) * 512);
        }
        if (i < 6) {
          c0[4 + 2 * i] = __builtin_amdgcn_exp2f(c0[4 + 2 * i]);
          c0[5 + 2 * i] = __builtin_amdgcn_exp2f(c0[5 + 2 * i]);
        } else if (i < 14) {
          c1[2 * i - 12] = __builtin_amdgcn_exp2f(c1[2 * i - 12]);
          c1[2 * i - 11] = __builtin_amdgcn_exp2f(c1[2 * i - 11]);
        }
        pw[i >> 2][i & 3] = i < 8 ? cvtpk(c0[2 * i], c0[2 * i + 1]) : cvtpk(c1[2 * i - 16], c1[2 * i - 15]);
        if (i >= 12) vf[i - 12] = tr2(vbp + (i - 12) * 512, 2048);
        SB();
      }
      float s0 = c0[0], s1 = c1[0], mx = 0.f;
#pragma unroll
      for (int i = 0; i < 16; ++i) {
        o[i & 3] = MFMA32(vf[i], __builtin_bit_cast(bf16x8, pw[i >> 2]), o[i & 3]);
        if (i < 12) vf[i + 4] = tr2(vbp + ((i + 4) >> 2) * 4096 + (i & 3) * 512, 2048);
        if (i > 0) {
          asm volatile("v_add_f32_e32 %0, %0, %1" : "+v"(s0) : "v"(c0[i]));
          asm volatile("v_add_f32_e32 %0, %0, %1" : "+v"(s1) : "v"(c1[i]));
        }
        if (doqk) {
          if (i == 1) mx = fmaxf(n0[0], n1[0]);
          if (i >= 1) mx = fmaxf(fmaxf(mx, n0[i]), n1[i]);
        }
        if ((i & 3) == 1) dma(i >> 2);
        SB();
      }
      ADV(wsl);
      l += s0 + s1;
      if (doqk) {
        if (__any(mx > THR)) {
          float rm = fmaxf(mx, swap32(mx));
          float delta = fmaxf(rm, 0.f);
          float alpha = __builtin_amdgcn_exp2f(-delta);
          m += delta;
          l *= alpha;
#pragma unroll
          for (int r = 0; r < 16; ++r) {
            n0[r] -= delta;
            n1[r] -= delta;
            negm[r] = -m;
          }
#pragma unroll
          for (int cc = 0; cc < 4; ++cc)
#pragma unroll
            for (int r = 0; r < 16; ++r) o[cc][r] *= alpha;
        }
      }
    };
    WAITBAR(12);
    wsl = wsl == 0 ? 4 : wsl - 1;
    astep(A0, A1, B0, B1, T_{}, [&](int) {});
    for (int j = 1; j < 11; j += 2) {
      WAITBAR(8);
      astep(B0, B1, A0, A1, T_{}, [&](int pc) { piece_kv(Kh, Vh, j + 4, wsl, pc); });
      WAITBAR(8);
      astep(A0, A1, B0, B1, T_{}, [&](int pc) { piece_kv(Kh, Vh, j + 5, wsl, pc); });
    }
    WAITBAR(8);
    astep(B0, B1, A0, A1, T_{}, [&](int pc) { piece_kv(Kh, Vh, 15, wsl, pc); });
    WAITBAR(8);
    astep(A0, A1, B0, B1, T_{}, [&](int pc) { if (item == 0) piece_wx(nwq, nx, 0, wsl, pc); });
    WAITBAR(4);
    astep(B0, B1, A0, A1, T_{}, [&](int pc) { if (item == 0) piece_wx(nwq, nx, 1, wsl, pc); });
    WAITBAR(0);
    astep(A0, A1, B0, B1, T_{}, [&](int pc) { if (item == 0) piece_wx(nwq, nx, 2, wsl, pc); });
    WAITBAR(0);
    astep(B0, B1, A0, A1, F_{}, [&](int pc) { if (item == 0) piece_wx(nwq, nx, 3, wsl, pc); });
    l += swap32(l);
    const float inv = 1.0f / l;
    unsigned short* dst = ows + ((((size_t)(item * 16 + b) * 4 + tb) * 32 + (2 * hkv + g)) * 8) * 512 + lane * 8;
#pragma unroll
    for (int cc = 0; cc < 4; ++cc) {
      *reinterpret_cast<bf16x8*>(dst + (cc * 2 + 0) * 512) = pack8(o[cc], 0, inv);
      *reinterpret_cast<bf16x8*>(dst + (cc * 2 + 1) * 512) = pack8(o[cc], 8, inv);
    }
  }
}

constexpr int ST2 = 26624;
#define MFMA16(a, b, c) __builtin_amdgcn_mfma_f32_16x16x32_bf16(a, b, c, 0, 0, 0)
__global__ __launch_bounds__(512, 2) void oproj_kernel(gptr ows, gptr woA, gptr woB, float* __restrict__ part) {
  extern __shared__ __attribute__((aligned(16))) char shm[];
  const int tid = threadIdx.x, lane = tid & 63;
  const int w = __builtin_amdgcn_readfirstlane(tid >> 6);
  const int tg = w >> 1, ch = w & 1;
  const int ii = blockIdx.x, xcd = ii & 7, jj = ii >> 3;
  const int prob = xcd >> 2, kq = xcd & 3, tbk = jj >> 1, cH = jj & 1;
  gptr wog = (prob ? woB : woA) + (size_t)(kq * 1024) * 576 + cH * 288;
  gptr og = ows + ((size_t)(prob * 64 + tbk * 4 + tg) * 128 + kq * 32) * 1024 + ch * 512;
  const unsigned ol = lane * 16;
  unsigned goff[3];
#pragma unroll
  for (int pc = 0; pc < 3; ++pc) {
    int L = (pc < 2 ? 2 * w + pc : 16 + (w & 1)) * 64 + lane, row = L / 36, sg = L % 36;
    goff[pc] = row * 1152 + (sg ^ (((row >> 2) & 1) << 1)) * 16;
  }
  const unsigned l0 = ldsaddr(shm);
  auto piece = [&](int st, int slot, int pc) {
    unsigned d = l0 + slot * ST2;
    if (pc == 0) glds(ol, og + (size_t)st * 1024, d + 18432 + w * 1024);
    else if (pc < 3) glds(goff[pc - 1], wog + (size_t)st * (32 * 576), d + (2 * w + pc - 1) * 1024);
    else if (w < 2) glds(goff[2], wog + (size_t)st * (32 * 576), d + (16 + w) * 1024);
  };
  const int gq = lane >> 4, g1 = gq & 1, q4 = (lane & 15) >> 2, p4 = lane & 3;
  const int abase = (16 * (gq >> 1) + 4 * g1 + q4) * 576 + ch * 288 + p4 * 8;
  const int a0 = abase + (ch ? -32 : 32) * g1, a1 = abase + (ch ? 32 : -32) * g1;
  const int ob = 18432 + tg * 2048 + (gq >> 1) * 1024 + (g1 * 32 + (lane & 15)) * 16;
  f32x4 acc[2][9] = {};
  bf16x8 fa[11], fb[11];
  int rs = 0;
  auto step = [&](bf16x8* Fn, bf16x8* Fc, auto domm, auto dma) {
    const char* s = shm + rs * ST2;
#pragma unroll
    for (int cb = 0; cb < 9; ++cb) {
      if (domm) {
        acc[0][cb] = MFMA16(Fc[cb], Fc[9], acc[0][cb]);
        acc[1][cb] = MFMA16(Fc[cb], Fc[10], acc[1][cb]);
      }
      if (cb == 0) {
        Fn[9] = ld128(s + ob);
        Fn[10] = ld128(s + ob + 256);
      }
      Fn[cb] = tr2(s + ((cb & 1) ? a1 : a0) + cb * 32, 4608);
      if (cb & 1) dma(cb >> 1);
      SB();
    }
    rs = rs == 5 ? 0 : rs + 1;
  };
  struct T_ { constexpr operator bool() const { return true; } };
  struct F_ { constexpr operator bool() const { return false; } };
#define WB2(n4, n3) do { if (w < 2) WAITBAR(n4); else WAITBAR(n3); } while (0)
  for (int st = 0; st < 5; ++st)
#pragma unroll
    for (int pc = 0; pc < 4; ++pc) piece(st, st, pc);
  WB2(16, 12);
  step(fa, fb, F_{}, [&](int pc) { piece(5, 5, pc); });
  for (int n = 1; n < 27; n += 2) {
    WB2(16, 12);
    step(fb, fa, T_{}, [&](int pc) { piece(n + 5, rs == 0 ? 5 : rs - 1, pc); });
    WB2(16, 12);
    step(fa, fb, T_{}, [&](int pc) { piece(n + 6, rs == 0 ? 5 : rs - 1, pc); });
  }
  WB2(16, 12);
  step(fb, fa, T_{}, [&](int) {});
  WB2(12, 9);
  step(fa, fb, T_{}, [&](int) {});
  WB2(8, 6);
  step(fb, fa, T_{}, [&](int) {});
  WB2(4, 3);
  step(fa, fb, T_{}, [&](int) {});
  WAITBAR(0);
  step(fb, fa, T_{}, [&](int) {});
#pragma unroll
  for (int cb = 0; cb < 9; ++cb) {
    acc[0][cb] = MFMA16(fb[cb], fb[9], acc[0][cb]);
    acc[1][cb] = MFMA16(fb[cb], fb[10], acc[1][cb]);
  }
  f32x4* dst = reinterpret_cast<f32x4*>(part) + ((size_t)(((kq * 2 + prob) * 16 + tbk) * 2 + cH) * 8 + w) * (18 * 64) + lane;
#pragma unroll
  for (int u = 0; u < 2; ++u)
#pragma unroll
    for (int cb = 0; cb < 9; ++cb) dst[(u * 9 + cb) * 64] = acc[u][cb];
}

constexpr int RB = 3;
__global__ __launch_bounds__(256) void reduce_kernel(const float* __restrict__ part, unsigned short* __restrict__ out) {
  const int gid = blockIdx.x * 256 + threadIdx.x, lane = gid & 63, wv = gid >> 6;
  const int bi = wv % (18 / RB), tw = wv / (18 / RB), w = tw & 7, tile = tw >> 3;
  const int cH = tile & 1, tbk = (tile >> 1) & 15, prob = tile >> 5;
  const f32x4* p = reinterpret_cast<const f32x4*>(part) + ((size_t)tw * 18 + bi * RB) * 64 + lane;
  constexpr size_t KQ = (size_t)64 * 8 * 18 * 64;
  f32x4 v[RB][4];
#pragma unroll
  for (int i = 0; i < RB; ++i)
#pragma unroll
    for (int k = 0; k < 4; ++k) v[i][k] = __builtin_nontemporal_load(p + i * 64 + k * KQ);
#pragma unroll
  for (int i = 0; i < RB; ++i) {
    const int blk = bi * RB + i, u = blk / 9, cb = blk % 9;
    f32x4 s = (v[i][0] + v[i][1]) + (v[i][2] + v[i][3]);
    const int tok = tbk * 128 + (w >> 1) * 32 + u * 16 + (lane & 15);
    const int col = prob * 576 + cH * 288 + (w & 1) * 144 + cb * 16 + 4 * (lane >> 4);
    u32x2 r = {cvtpk(s[0], s[1]), cvtpk(s[2], s[3])};
    *reinterpret_cast<u32x2*>(out + (size_t)tok * 1152 + col) = r;
  }
}

extern "C" void kernel_launch(void* const* d_in, const int* in_sizes, int n_in, void* d_out, int out_size, void* d_ws,
                              size_t ws_size, hipStream_t stream) {
  (void)in_sizes; (void)n_in; (void)out_size; (void)ws_size;
  gptr xA = (gptr)d_in[0], kA = (gptr)d_in[1], vA = (gptr)d_in[2], wqA = (gptr)d_in[3], woA = (gptr)d_in[4];
  gptr xB = (gptr)d_in[5], kB = (gptr)d_in[6], vB = (gptr)d_in[7], wqB = (gptr)d_in[8], woB = (gptr)d_in[9];
  unsigned short* ows = (unsigned short*)d_ws;
  float* part = (float*)((char*)d_ws + ((size_t)1 << 25));
  hipFuncSetAttribute((const void*)attn_kernel, hipFuncAttributeMaxDynamicSharedMemorySize, 163840);
  hipFuncSetAttribute((const void*)oproj_kernel, hipFuncAttributeMaxDynamicSharedMemorySize, 163840);
  hipLaunchKernelGGL(attn_kernel, dim3(256), dim3(512), 163840, stream, xA, kA, vA, wqA, xB, kB, vB, wqB, ows);
  hipLaunchKernelGGL(oproj_kernel, dim3(256), dim3(512), 6 * ST2, stream, (gptr)ows, woA, woB, part);
  hipLaunchKernelGGL(reduce_kernel, dim3(2304 / RB), dim3(256), 0, stream, (const float*)part, (unsigned short*)d_out);
}
```

```cpp
#include <hip/hip_runtime.h>
#include <hip/hip_bf16.h>
#include <stdint.h>

typedef __attribute__((ext_vector_type(8))) short bf16x8;
typedef __attribute__((ext_vector_type(4))) short s16x4;
typedef __attribute__((ext_vector_type(16))) float f32x16;
typedef __attribute__((ext_vector_type(4))) float f32x4;
typedef __attribute__((ext_vector_type(2))) float f32x2;
typedef __attribute__((ext_vector_type(2))) __bf16 bf16x2;
typedef __attribute__((ext_vector_type(4))) unsigned u32x4;
typedef __attribute__((ext_vector_type(2))) unsigned u32x2;
typedef __attribute__((address_space(3))) s16x4* lds_s16x4_p;
typedef __attribute__((address_space(3))) void* lds_void_p;
typedef const unsigned short* gptr;

#define DEV __device__ __forceinline__

DEV unsigned cvtpk(float lo, float hi) {
  f32x2 v = {lo, hi};
  bf16x2 r = __builtin_convertvector(v, bf16x2);
  return __builtin_bit_cast(unsigned, r);
}
DEV bf16x8 pack8(const f32x16& a, int base, float sc) {
  u32x4 w = {cvtpk(a[base + 0] * sc, a[base + 1] * sc), cvtpk(a[base + 2] * sc, a[base + 3] * sc),
             cvtpk(a[base + 4] * sc, a[base + 5] * sc), cvtpk(a[base + 6] * sc, a[base + 7] * sc)};
  return __builtin_bit_cast(bf16x8, w);
}
DEV bf16x8 pack8n(const f32x16& a, int base) {
  u32x4 w = {cvtpk(a[base + 0], a[base + 1]), cvtpk(a[base + 2], a[base + 3]),
             cvtpk(a[base + 4], a[base + 5]), cvtpk(a[base + 6], a[base + 7])};
  return __builtin_bit_cast(bf16x8, w);
}
DEV void glds(unsigned voff, const void* sbase, unsigned lds) {
  unsigned keep;
  asm volatile("s_mov_b32 %0, m0\n\ts_mov_b32 m0, %3\n\ts_nop 0\n\tglobal_load_lds_dwordx4 %1, %2\n\ts_mov_b32 m0, %0"
               : "=&s"(keep) : "v"(voff), "s"(sbase), "s"(lds) : "memory");
}
DEV void glds_nt(unsigned voff, const void* sbase, unsigned lds) {
  unsigned keep;
  asm volatile("s_mov_b32 %0, m0\n\ts_mov_b32 m0, %3\n\ts_nop 0\n\tglobal_load_lds_dwordx4 %1, %2 nt\n\ts_mov_b32 m0, %0"
               : "=&s"(keep) : "v"(voff), "s"(sbase), "s"(lds) : "memory");
}
DEV unsigned ldsaddr(const char* p) { return (unsigned)(size_t)(lds_void_p)p; }
#define SB() __builtin_amdgcn_sched_barrier(0)
DEV s16x4 trrd(const char* l) { return __builtin_amdgcn_ds_read_tr16_b64_v4i16((lds_s16x4_p)l); }
DEV bf16x8 tr2(const char* l, int d) {
  s16x4 a = trrd(l), b = trrd(l + d);
  return __builtin_shufflevector(a, b, 0, 1, 2, 3, 4, 5, 6, 7);
}
DEV bf16x8 ld128(const char* l) { return *reinterpret_cast<const bf16x8*>(l); }
DEV float swap32(float x) {
  auto rr = __builtin_amdgcn_permlane32_swap(__float_as_uint(x), __float_as_uint(x), false, false);
  return __uint_as_float((threadIdx.x & 32) ? rr[0] : rr[1]);
}
#define WAITBAR(n)                                                          \
  do {                                                                      \
    __builtin_amdgcn_s_waitcnt(0xC07F);                      \
    asm volatile("s_waitcnt vmcnt(" #n ")\n\ts_barrier" ::: "memory");     \
  } while (0)

constexpr int SLOT = 32768;
constexpr float QSCALE = 0.088388347648318447f * 1.4426950408889634f;
constexpr float THR = 8.0f;

#define MFMA32(a, b, c) __builtin_amdgcn_mfma_f32_32x32x16_bf16(a, b, c, 0, 0, 0)
__global__ __launch_bounds__(512, 2) void attn_kernel(gptr xA, gptr kA, gptr vA, gptr wqA, gptr xB, gptr kB, gptr vB,
                                                      gptr wqB, unsigned short* __restrict__ ows) {
  extern __shared__ __attribute__((aligned(16))) char shm[];
  const int tid = threadIdx.x, lane = tid & 63;
  const int w = __builtin_amdgcn_readfirstlane(tid >> 6);
  const int ii = blockIdx.x, xcd = ii & 7, jj = ii >> 3;
  const int hkv = 2 * xcd + (jj & 1), b = jj >> 1;
  const int g = w >> 2, tb = w & 3;
  const int h = lane >> 5, l31 = lane & 31;
  const size_t kvoff = (size_t)(b * 16 + hkv) * (1024 * 128);

  const int sub = lane >> 5, row7 = (lane & 31) >> 2, slot4 = lane & 3;
  const unsigned kg = 2 * ((8 * w + row7) * 128 + sub * 32 + ((slot4 ^ ((2 * w + (row7 >> 2)) & 3)) * 8));
  const unsigned vg = 2 * ((8 * w + row7) * 128 + sub * 32 + slot4 * 8);
  const unsigned wg = 2 * ((8 * (w >> 1) + row7) * 4096 + ((2 * w) & 3) * 64 + sub * 32 + slot4 * 8);
  const unsigned xg = 2 * ((16 * w + (lane >> 2)) * 576 + ((slot4 ^ ((lane >> 4) & 3)) * 8));
  const unsigned l0 = ldsaddr(shm);

  auto piece_kv = [&](gptr Kh, gptr Vh, int t, int slot, int pc) {
    unsigned dst = l0 + slot * SLOT + w * 2048;
    if (pc == 0) glds_nt(kg, Kh + t * 8192, dst);
    if (pc == 1) glds_nt(kg, Kh + t * 8192 + 64, dst + 1024);
    if (pc == 2) glds_nt(vg, Vh + t * 8192, dst + 16384);
    if (pc == 3) glds_nt(vg, Vh + t * 8192 + 64, dst + 16384 + 1024);
  };
  auto piece_wx = [&](gptr wqh, gptr xbp, int c, int slot, int pc) {
    unsigned base = l0 + slot * SLOT;
    gptr ws_ = wqh + (size_t)c * (32 * 4096);
    if (pc == 0) glds(wg, ws_, base + w * 2048);
    if (pc == 1) glds(wg, ws_ + 64, base + w * 2048 + 1024);
    if (pc == 2) glds(xg, xbp + c * 32, base + 16384 + w * 1024);
  };
  auto issue_wx = [&](gptr wqh, gptr xbp, int c, int slot) {
    piece_wx(wqh, xbp, c, slot, 0);
    piece_wx(wqh, xbp, c, slot, 1);
    piece_wx(wqh, xbp, c, slot, 2);
  };

  const int q4 = (lane & 15) >> 2, p4 = lane & 3, blk = (lane >> 4) & 1;
  const int p4s = ((p4 & 1) << 1) | (p4 >> 1);
  const int wbase = h * 4096 + g * 2048 + q4 * 64 + blk * 32 + p4s * 8;
  const int xrow = 32 * tb + l31;
  const int xb0 = 16384 + 512 * (xrow >> 3) + 64 * (xrow & 7) + 16 * ((0 + h) ^ ((xrow >> 2) & 3));
  const int xb1 = 16384 + 512 * (xrow >> 3) + 64 * (xrow & 7) + 16 * ((2 + h) ^ ((xrow >> 2) & 3));
  const int kb0 = 2048 * (l31 >> 3) + 64 * (l31 & 7) + 16 * ((0 + h) ^ ((l31 >> 2) & 3));
  const int kb1 = 2048 * (l31 >> 3) + 64 * (l31 & 7) + 16 * ((2 + h) ^ ((l31 >> 2) & 3));
  const int vb = 16384 + h * 256 + q4 * 64 + blk * 32 + p4 * 8;

  int rs = 0, wsl = 4;
#define ADV(x) x = (x == 4 ? 0 : x + 1)
  struct T_ { constexpr operator bool() const { return true; } };
  struct F_ { constexpr operator bool() const { return false; } };
  {
    gptr wqh = wqA + hkv * 256, xbp = xA + (size_t)b * (128 * 576);
    issue_wx(wqh, xbp, 0, 0);
    issue_wx(wqh, xbp, 1, 1);
    issue_wx(wqh, xbp, 2, 2);
    issue_wx(wqh, xbp, 3, 3);
  }
  gptr nwq = wqB + hkv * 256, nx = xB + (size_t)b * (128 * 576);
  for (int item = 0; item < 2; ++item) {
    gptr wqh = (item ? wqB : wqA) + hkv * 256, xbp = (item ? xB : xA) + (size_t)b * (128 * 576);
    gptr Kh = (item ? kB : kA) + kvoff, Vh = (item ? vB : vA) + kvoff;
    f32x16 acc[4] = {};
    bf16x8 fa[10], fb[10];
    auto qstep = [&](bf16x8* Fn, bf16x8* Fc, auto domm, auto dma) {
      const char* base = shm + rs * SLOT;
      ADV(rs);
#pragma unroll
      for (int i = 0; i < 8; ++i) {
        if (domm) acc[i & 3] = MFMA32(Fc[i], Fc[8 + (i >> 2)], acc[i & 3]);
        if (i == 0) {
          Fn[8] = ld128(base + xb0);
          Fn[9] = ld128(base + xb1);
        }
        Fn[i] = tr2(base + wbase + (i >> 2) * 8192 + (i & 3) * 512, 256);
        if (i & 1) dma(i >> 1);
        SB();
      }
      ADV(wsl);
    };
    WAITBAR(9);
    qstep(fa, fb, F_{}, [&](int pc) { piece_wx(wqh, xbp, 4, wsl, pc); });
    for (int c = 0; c < 12; c += 2) {
      WAITBAR(9);
      qstep(fb, fa, T_{}, [&](int pc) { piece_wx(wqh, xbp, c + 5, wsl, pc); });
      WAITBAR(9);
      qstep(fa, fb, T_{}, [&](int pc) { piece_wx(wqh, xbp, c + 6, wsl, pc); });
    }
    WAITBAR(9);
    qstep(fb, fa, T_{}, [&](int pc) { piece_wx(wqh, xbp, 17, wsl, pc); });
    for (int c = 0; c < 4; c += 2) {
      WAITBAR(9);
      qstep(fa, fb, T_{}, [&](int pc) { piece_kv(Kh, Vh, c, wsl, pc); });
      WAITBAR(9);
      qstep(fb, fa, T_{}, [&](int pc) { piece_kv(Kh, Vh, c + 1, wsl, pc); });
    }
    WAITBAR(9);
#pragma unroll
    for (int i = 0; i < 8; ++i) {
      acc[i & 3] = MFMA32(fb[i], fb[8 + (i >> 2)], acc[i & 3]);
      if (i & 1) piece_kv(Kh, Vh, 4, wsl, i >> 1);
      SB();
    }
    ADV(wsl);
    bf16x8 qf[4][2];
#pragma unroll
    for (int cc = 0; cc < 4; ++cc) {
      qf[cc][0] = pack8(acc[cc], 0, QSCALE);
      qf[cc][1] = pack8(acc[cc], 8, QSCALE);
    }

    f32x16 o[4] = {};
    float m, l = 0.f;
    f32x16 negm = {};
    f32x16 A0, A1, B0, B1;
    {
      const char* kp0 = shm + rs * SLOT + kb0;
      const char* kp1 = shm + rs * SLOT + kb1;
      bf16x8 kf[16];
#pragma unroll
      for (int i = 0; i < 16; ++i) kf[i] = ld128(((i & 2) ? kp1 : kp0) + (i & 1) * 8192 + (i >> 2) * 512);
#pragma unroll
      for (int i = 0; i < 16; ++i) {
        if (i & 1) A1 = MFMA32(kf[i], qf[i >> 2][(i >> 1) & 1], i < 2 ? negm : A1);
        else A0 = MFMA32(kf[i], qf[i >> 2][(i >> 1) & 1], i < 2 ? negm : A0);
      }
      float mx = fmaxf(A0[0], A1[0]);
#pragma unroll
      for (int r = 1; r < 16; ++r) mx = fmaxf(fmaxf(mx, A0[r]), A1[r]);
      m = fmaxf(mx, swap32(mx));
#pragma unroll
      for (int r = 0; r < 16; ++r) {
        A0[r] -= m;
        A1[r] -= m;
        negm[r] = -m;
      }
      SB();
    }
    auto astep = [&](f32x16& c0, f32x16& c1, f32x16& n0, f32x16& n1, auto doqk, auto dma) {
      const char* vbp = shm + rs * SLOT + vb;
      ADV(rs);
      const char* kp0 = shm + rs * SLOT + kb0;
      const char* kp1 = shm + rs * SLOT + kb1;
      bf16x8 kf[16], vf[16];
      u32x4 pw[4];
      if (doqk) {
#pragma unroll
        for (int i = 0; i < 4; ++i) kf[i] = ld128(((i & 2) ? kp1 : kp0) + (i & 1) * 8192 + (i >> 2) * 512);
      }
#pragma unroll
      for (int r = 0; r < 4; ++r) c0[r] = __builtin_amdgcn_exp2f(c0[r]);
      SB();
#pragma unroll
      for (int i = 0; i < 16; ++i) {
        if (doqk) {
          if (i & 1) n1 = MFMA32(kf[i], qf[i >> 2][(i >> 1) & 1], i < 2 ? negm : n1);
          else n0 = MFMA32(kf[i], qf[i >> 2][(i >> 1) & 1], i < 2 ? negm : n0);
          if (i < 12) kf[i + 4] = ld128((((i + 4) & 2) ? kp1 : kp0) + (i & 1) * 8192 + ((i + 4) >> 2) * 512);
        }
        if (i < 6) {
          c0[4 + 2 * i] = __builtin_amdgcn_exp2f(c0[4 + 2 * i]);
          c0[5 + 2 * i] = __builtin_amdgcn_exp2f(c0[5 + 2 * i]);
        } else if (i < 14) {
          c1[2 * i - 12] = __builtin_amdgcn_exp2f(c1[2 * i - 12]);
          c1[2 * i - 11] = __builtin_amdgcn_exp2f(c1[2 * i - 11]);
        }
        pw[i >> 2][i & 3] = i < 8 ? cvtpk(c0[2 * i], c0[2 * i + 1]) : cvtpk(c1[2 * i - 16], c1[2 * i - 15]);
        if (i >= 12) vf[i - 12] = tr2(vbp + (i - 12) * 512, 2048);
        SB();
      }
      float s0 = c0[0], s1 = c1[0], mx = 0.f;
#pragma unroll
      for (int i = 0; i < 16; ++i) {
        o[i & 3] = MFMA32(vf[i], __builtin_bit_cast(bf16x8, pw[i >> 2]), o[i & 3]);
        if (i < 12) vf[i + 4] = tr2(vbp + ((i + 4) >> 2) * 4096 + (i & 3) * 512, 2048);
        if (i > 0) {
          asm volatile("v_add_f32_e32 %0, %0, %1" : "+v"(s0) : "v"(c0[i]));
          asm volatile("v_add_f32_e32 %0, %0, %1" : "+v"(s1) : "v"(c1[i]));
        }
        if (doqk) {
          if (i == 1) mx = fmaxf(n0[0], n1[0]);
          if (i >= 1) mx = fmaxf(fmaxf(mx, n0[i]), n1[i]);
        }
        if ((i & 3) == 1) dma(i >> 2);
        SB();
      }
      ADV(wsl);
      l += s0 + s1;
      if (doqk) {
        if (__any(mx > THR)) {
          float rm = fmaxf(mx, swap32(mx));
          float delta = fmaxf(rm, 0.f);
          float alpha = __builtin_amdgcn_exp2f(-delta);
          m += delta;
          l *= alpha;
#pragma unroll
          for (int r = 0; r < 16; ++r) {
            n0[r] -= delta;
            n1[r] -= delta;
            negm[r] = -m;
          }
#pragma unroll
          for (int cc = 0; cc < 4; ++cc)
#pragma unroll
            for (int r = 0; r < 16; ++r) o[cc][r] *= alpha;
        }
      }
    };
    WAITBAR(12);
    wsl = wsl == 0 ? 4 : wsl - 1;
    astep(A0, A1, B0, B1, T_{}, [&](int) {});
    for (int j = 1; j < 11; j += 2) {
      WAITBAR(8);
      astep(B0, B1, A0, A1, T_{}, [&](int pc) { piece_kv(Kh, Vh, j + 4, wsl, pc); });
      WAITBAR(8);
      astep(A0, A1, B0, B1, T_{}, [&](int pc) { piece_kv(Kh, Vh, j + 5, wsl, pc); });
    }
    WAITBAR(8);
    astep(B0, B1, A0, A1, T_{}, [&](int pc) { piece_kv(Kh, Vh, 15, wsl, pc); });
    WAITBAR(8);
    astep(A0, A1, B0, B1, T_{}, [&](int pc) { if (item == 0) piece_wx(nwq, nx, 0, wsl, pc); });
    WAITBAR(4);
    astep(B0, B1, A0, A1, T_{}, [&](int pc) { if (item == 0) piece_wx(nwq, nx, 1, wsl, pc); });
    WAITBAR(0);
    astep(A0, A1, B0, B1, T_{}, [&](int pc) { if (item == 0) piece_wx(nwq, nx, 2, wsl, pc); });
    WAITBAR(0);
    astep(B0, B1, A0, A1, F_{}, [&](int pc) { if (item == 0) piece_wx(nwq, nx, 3, wsl, pc); });
    l += swap32(l);
    const float inv = 1.0f / l;
    unsigned short* dst = ows + ((((size_t)(item * 16 + b) * 4 + tb) * 32 + (2 * hkv + g)) * 8) * 512 + lane * 8;
#pragma unroll
    for (int cc = 0; cc < 4; ++cc) {
      __builtin_nontemporal_store(pack8(o[cc], 0, inv), reinterpret_cast<bf16x8*>(dst + (cc * 2 + 0) * 512));
      __builtin_nontemporal_store(pack8(o[cc], 8, inv), reinterpret_cast<bf16x8*>(dst + (cc * 2 + 1) * 512));
    }
  }
}

constexpr int ST2 = 26624;
#define MFMA16(a, b, c) __builtin_amdgcn_mfma_f32_16x16x32_bf16(a, b, c, 0, 0, 0)
__global__ __launch_bounds__(512, 2) void oproj_kernel(gptr ows, gptr woA, gptr woB, float* __restrict__ part) {
  extern __shared__ __attribute__((aligned(16))) char shm[];
  const int tid = threadIdx.x, lane = tid & 63;
  const int w = __builtin_amdgcn_readfirstlane(tid >> 6);
  const int tg = w >> 1, ch = w & 1;
  const int ii = blockIdx.x, xcd = ii & 7, jj = ii >> 3;
  const int prob = xcd >> 2, kq = xcd & 3, tbk = jj >> 1, cH = jj & 1;
  gptr wog = (prob ? woB : woA) + (size_t)(kq * 1024) * 576 + cH * 288;
  gptr og = ows + ((size_t)(prob * 64 + tbk * 4 + tg) * 128 + kq * 32) * 1024 + ch * 512;
  const unsigned ol = lane * 16;
  unsigned goff[3];
#pragma unroll
  for (int pc = 0; pc < 3; ++pc) {
    int L = (pc < 2 ? 2 * w + pc : 16 + (w & 1)) * 64 + lane, row = L / 36, sg = L % 36;
    goff[pc] = row * 1152 + (sg ^ (((row >> 2) & 1) << 1)) * 16;
  }
  const unsigned l0 = ldsaddr(shm);
  auto piece = [&](int st, int slot, int pc) {
    unsigned d = l0 + slot * ST2;
    if (pc == 0) glds(ol, og + (size_t)st * 1024, d + 18432 + w * 1024);
    else if (pc < 3) glds(goff[pc - 1], wog + (size_t)st * (32 * 576), d + (2 * w + pc - 1) * 1024);
    else if (w < 2) glds(goff[2], wog + (size_t)st * (32 * 576), d + (16 + w) * 1024);
  };
  const int gq = lane >> 4, g1 = gq & 1, q4 = (lane & 15) >> 2, p4 = lane & 3;
  const int abase = (16 * (gq >> 1) + 4 * g1 + q4) * 576 + ch * 288 + p4 * 8;
  const int a0 = abase + (ch ? -32 : 32) * g1, a1 = abase + (ch ? 32 : -32) * g1;
  const int ob = 18432 + tg * 2048 + (gq >> 1) * 1024 + (g1 * 32 + (lane & 15)) * 16;
  f32x4 acc[2][9] = {};
  bf16x8 fa[11], fb[11];
  int rs = 0;
  auto step = [&](bf16x8* Fn, bf16x8* Fc, auto domm, auto dma) {
    const char* s = shm + rs * ST2;
#pragma unroll
    for (int cb = 0; cb < 9; ++cb) {
      if (domm) {
        acc[0][cb] = MFMA16(Fc[cb], Fc[9], acc[0][cb]);
        acc[1][cb] = MFMA16(Fc[cb], Fc[10], acc[1][cb]);
      }
      if (cb == 0) {
        Fn[9] = ld128(s + ob);
        Fn[10] = ld128(s + ob + 256);
      }
      Fn[cb] = tr2(s + ((cb & 1) ? a1 : a0) + cb * 32, 4608);
      if (cb & 1) dma(cb >> 1);
      SB();
    }
    rs = rs == 5 ? 0 : rs + 1;
  };
  struct T_ { constexpr operator bool() const { return true; } };
  struct F_ { constexpr operator bool() const { return false; } };
#define WB2(n4, n3) do { if (w < 2) WAITBAR(n4); else WAITBAR(n3); } while (0)
  for (int st = 0; st < 5; ++st)
#pragma unroll
    for (int pc = 0; pc < 4; ++pc) piece(st, st, pc);
  WB2(16, 12);
  step(fa, fb, F_{}, [&](int pc) { piece(5, 5, pc); });
  for (int n = 1; n < 27; n += 2) {
    WB2(16, 12);
    step(fb, fa, T_{}, [&](int pc) { piece(n + 5, rs == 0 ? 5 : rs - 1, pc); });
    WB2(16, 12);
    step(fa, fb, T_{}, [&](int pc) { piece(n + 6, rs == 0 ? 5 : rs - 1, pc); });
  }
  WB2(16, 12);
  step(fb, fa, T_{}, [&](int) {});
  WB2(12, 9);
  step(fa, fb, T_{}, [&](int) {});
  WB2(8, 6);
  step(fb, fa, T_{}, [&](int) {});
  WB2(4, 3);
  step(fa, fb, T_{}, [&](int) {});
  WAITBAR(0);
  step(fb, fa, T_{}, [&](int) {});
#pragma unroll
  for (int cb = 0; cb < 9; ++cb) {
    acc[0][cb] = MFMA16(fb[cb], fb[9], acc[0][cb]);
    acc[1][cb] = MFMA16(fb[cb], fb[10], acc[1][cb]);
  }
  f32x4* dst = reinterpret_cast<f32x4*>(part) + ((size_t)(((kq * 2 + prob) * 16 + tbk) * 2 + cH) * 8 + w) * (18 * 64) + lane;
#pragma unroll
  for (int u = 0; u < 2; ++u)
#pragma unroll
    for (int cb = 0; cb < 9; ++cb) __builtin_nontemporal_store(acc[u][cb], dst + (u * 9 + cb) * 64);
}

constexpr int RB = 3;
__global__ __launch_bounds__(256) void reduce_kernel(const float* __restrict__ part, unsigned short* __restrict__ out) {
  const int gid = blockIdx.x * 256 + threadIdx.x, lane = gid & 63, wv = gid >> 6;
  const int bi = wv % (18 / RB), tw = wv / (18 / RB), w = tw & 7, tile = tw >> 3;
  const int cH = tile & 1, tbk = (tile >> 1) & 15, prob = tile >> 5;
  const f32x4* p = reinterpret_cast<const f32x4*>(part) + ((size_t)tw * 18 + bi * RB) * 64 + lane;
  constexpr size_t KQ = (size_t)64 * 8 * 18 * 64;
  f32x4 v[RB][4];
#pragma unroll
  for (int i = 0; i < RB; ++i)
#pragma unroll
    for (int k = 0; k < 4; ++k) v[i][k] = __builtin_nontemporal_load(p + i * 64 + k * KQ);
#pragma unroll
  for (int i = 0; i < RB; ++i) {
    const int blk = bi * RB + i, u = blk / 9, cb = blk % 9;
    f32x4 s = (v[i][0] + v[i][1]) + (v[i][2] + v[i][3]);
    const int tok = tbk * 128 + (w >> 1) * 32 + u * 16 + (lane & 15);
    const int col = prob * 576 + cH * 288 + (w & 1) * 144 + cb * 16 + 4 * (lane >> 4);
    u32x2 r = {cvtpk(s[0], s[1]), cvtpk(s[2], s[3])};
    __builtin_nontemporal_store(r, reinterpret_cast<u32x2*>(out + (size_t)tok * 1152 + col));
  }
}

extern "C" void kernel_launch(void* const* d_in, const int* in_sizes, int n_in, void* d_out, int out_size, void* d_ws,
                              size_t ws_size, hipStream_t stream) {
  (void)in_sizes; (void)n_in; (void)out_size; (void)ws_size;
  gptr xA = (gptr)d_in[0], kA = (gptr)d_in[1], vA = (gptr)d_in[2], wqA = (gptr)d_in[3], woA = (gptr)d_in[4];
  gptr xB = (gptr)d_in[5], kB = (gptr)d_in[6], vB = (gptr)d_in[7], wqB = (gptr)d_in[8], woB = (gptr)d_in[9];
  unsigned short* ows = (unsigned short*)d_ws;
  float* part = (float*)((char*)d_ws + ((size_t)1 << 25));
  hipFuncSetAttribute((const void*)attn_kernel, hipFuncAttributeMaxDynamicSharedMemorySize, 163840);
  hipFuncSetAttribute((const void*)oproj_kernel, hipFuncAttributeMaxDynamicSharedMemorySize, 163840);
  hipLaunchKernelGGL(attn_kernel, dim3(256), dim3(512), 163840, stream, xA, kA, vA, wqA, xB, kB, vB, wqB, ows);
  hipLaunchKernelGGL(oproj_kernel, dim3(256), dim3(512), 6 * ST2, stream, (gptr)ows, woA, woB, part);
  hipLaunchKernelGGL(reduce_kernel, dim3(2304 / RB), dim3(256), 0, stream, (const float*)part, (unsigned short*)d_out);
}
```

```cpp
#include <hip/hip_runtime.h>
#include <hip/hip_bf16.h>
#include <stdint.h>

typedef __attribute__((ext_vector_type(8))) short bf16x8;
typedef __attribute__((ext_vector_type(4))) short s16x4;
typedef __attribute__((ext_vector_type(16))) float f32x16;
typedef __attribute__((ext_vector_type(4))) float f32x4;
typedef __attribute__((ext_vector_type(2))) float f32x2;
typedef __attribute__((ext_vector_type(2))) __bf16 bf16x2;
typedef __attribute__((ext_vector_type(4))) unsigned u32x4;
typedef __attribute__((ext_vector_type(2))) unsigned u32x2;
typedef __attribute__((address_space(3))) s16x4* lds_s16x4_p;
typedef __attribute__((address_space(3))) void* lds_void_p;
typedef const unsigned short* gptr;

#define DEV __device__ __forceinline__

DEV unsigned cvtpk(float lo, float hi) {
  f32x2 v = {lo, hi};
  bf16x2 r = __builtin_convertvector(v, bf16x2);
  return __builtin_bit_cast(unsigned, r);
}
DEV bf16x8 pack8(const f32x16& a, int base, float sc) {
  u32x4 w = {cvtpk(a[base + 0] * sc, a[base + 1] * sc), cvtpk(a[base + 2] * sc, a[base + 3] * sc),
             cvtpk(a[base + 4] * sc, a[base + 5] * sc), cvtpk(a[base + 6] * sc, a[base + 7] * sc)};
  return __builtin_bit_cast(bf16x8, w);
}
DEV bf16x8 pack8n(const f32x16& a, int base) {
  u32x4 w = {cvtpk(a[base + 0], a[base + 1]), cvtpk(a[base + 2], a[base + 3]),
             cvtpk(a[base + 4], a[base + 5]), cvtpk(a[base + 6], a[base + 7])};
  return __builtin_bit_cast(bf16x8, w);
}
DEV void glds(unsigned voff, const void* sbase, unsigned lds) {
  unsigned keep;
  asm volatile("s_mov_b32 %0, m0\n\ts_mov_b32 m0, %3\n\ts_nop 0\n\tglobal_load_lds_dwordx4 %1, %2\n\ts_mov_b32 m0, %0"
               : "=&s"(keep) : "v"(voff), "s"(sbase), "s"(lds) : "memory");
}
DEV void glds_nt(unsigned voff, const void* sbase, unsigned lds) {
  unsigned keep;
  asm volatile("s_mov_b32 %0, m0\n\ts_mov_b32 m0, %3\n\ts_nop 0\n\tglobal_load_lds_dwordx4 %1, %2 nt\n\ts_mov_b32 m0, %0"
               : "=&s"(keep) : "v"(voff), "s"(sbase), "s"(lds) : "memory");
}
DEV unsigned ldsaddr(const char* p) { return (unsigned)(size_t)(lds_void_p)p; }
#define SB() __builtin_amdgcn_sched_barrier(0)
DEV s16x4 trrd(const char* l) { return __builtin_amdgcn_ds_read_tr16_b64_v4i16((lds_s16x4_p)l); }
DEV bf16x8 tr2(const char* l, int d) {
  s16x4 a = trrd(l), b = trrd(l + d);
  return __builtin_shufflevector(a, b, 0, 1, 2, 3, 4, 5, 6, 7);
}
DEV bf16x8 ld128(const char* l) { return *reinterpret_cast<const bf16x8*>(l); }
DEV float swap32(float x) {
  auto rr = __builtin_amdgcn_permlane32_swap(__float_as_uint(x), __float_as_uint(x), false, false);
  return __uint_as_float((threadIdx.x & 32) ? rr[0] : rr[1]);
}
#define WAITBAR(n)                                                          \
  do {                                                                      \
    __builtin_amdgcn_s_waitcnt(0xC07F);                      \
    asm volatile("s_waitcnt vmcnt(" #n ")\n\ts_barrier" ::: "memory");     \
  } while (0)

constexpr int SLOT = 32768;
constexpr float QSCALE = 0.088388347648318447f * 1.4426950408889634f;
constexpr float THR = 8.0f;

#define MFMA32(a, b, c) __builtin_amdgcn_mfma_f32_32x32x16_bf16(a, b, c, 0, 0, 0)
__global__ __launch_bounds__(512, 2) void attn_kernel(gptr xA, gptr kA, gptr vA, gptr wqA, gptr xB, gptr kB, gptr vB,
                                                      gptr wqB, unsigned short* __restrict__ ows) {
  extern __shared__ __attribute__((aligned(16))) char shm[];
  const int tid = threadIdx.x, lane = tid & 63;
  const int w = __builtin_amdgcn_readfirstlane(tid >> 6);
  const int ii = blockIdx.x, xcd = ii & 7, jj = ii >> 3;
  const int hkv = 2 * xcd + (jj & 1), b = jj >> 1;
  const int g = w >> 2, tb = w & 3;
  const int h = lane >> 5, l31 = lane & 31;
  const size_t kvoff = (size_t)(b * 16 + hkv) * (1024 * 128);

  const int sub = lane >> 5, row7 = (lane & 31) >> 2, slot4 = lane & 3;
  const unsigned kg = 2 * ((8 * w + row7) * 128 + sub * 32 + ((slot4 ^ ((2 * w + (row7 >> 2)) & 3)) * 8));
  const unsigned vg = 2 * ((8 * w + row7) * 128 + sub * 32 + slot4 * 8);
  const unsigned wg = 2 * ((8 * (w >> 1) + row7) * 4096 + ((2 * w) & 3) * 64 + sub * 32 + slot4 * 8);
  const unsigned xg = 2 * ((16 * w + (lane >> 2)) * 576 + ((slot4 ^ ((lane >> 4) & 3)) * 8));
  const unsigned l0 = ldsaddr(shm);

  auto piece_kv = [&](gptr Kh, gptr Vh, int t, int slot, int pc) {
    unsigned dst = l0 + slot * SLOT + w * 2048;
    if (pc == 0) glds_nt(kg, Kh + t * 8192, dst);
    if (pc == 1) glds_nt(kg, Kh + t * 8192 + 64, dst + 1024);
    if (pc == 2) glds_nt(vg, Vh + t * 8192, dst + 16384);
    if (pc == 3) glds_nt(vg, Vh + t * 8192 + 64, dst + 16384 + 1024);
  };
  auto piece_wx = [&](gptr wqh, gptr xbp, int c, int slot, int pc) {
    unsigned base = l0 + slot * SLOT;
    gptr ws_ = wqh + (size_t)c * (32 * 4096);
    if (pc == 0) glds(wg, ws_, base + w * 2048);
    if (pc == 1) glds(wg, ws_ + 64, base + w * 2048 + 1024);
    if (pc == 2) glds(xg, xbp + c * 32, base + 16384 + w * 1024);
  };
  auto issue_wx = [&](gptr wqh, gptr xbp, int c, int slot) {
    piece_wx(wqh, xbp, c, slot, 0);
    piece_wx(wqh, xbp, c, slot, 1);
    piece_wx(wqh, xbp, c, slot, 2);
  };

  const int q4 = (lane & 15) >> 2, p4 = lane & 3, blk = (lane >> 4) & 1;
  const int p4s = ((p4 & 1) << 1) | (p4 >> 1);
  const int wbase = h * 4096 + g * 2048 + q4 * 64 + blk * 32 + p4s * 8;
  const int xrow = 32 * tb + l31;
  const int xb0 = 16384 + 512 * (xrow >> 3) + 64 * (xrow & 7) + 16 * ((0 + h) ^ ((xrow >> 2) & 3));
  const int xb1 = 16384 + 512 * (xrow >> 3) + 64 * (xrow & 7) + 16 * ((2 + h) ^ ((xrow >> 2) & 3));
  const int kb0 = 2048 * (l31 >> 3) + 64 * (l31 & 7) + 16 * ((0 + h) ^ ((l31 >> 2) & 3));
  const int kb1 = 2048 * (l31 >> 3) + 64 * (l31 & 7) + 16 * ((2 + h) ^ ((l31 >> 2) & 3));
  const int vb = 16384 + h * 256 + q4 * 64 + blk * 32 + p4 * 8;

  int rs = 0, wsl = 4;
#define ADV(x) x = (x == 4 ? 0 : x + 1)
  struct T_ { constexpr operator bool() const { return true; } };
  struct F_ { constexpr operator bool() const { return false; } };
  {
    gptr wqh = wqA + hkv * 256, xbp = xA + (size_t)b * (128 * 576);
    issue_wx(wqh, xbp, 0, 0);
    issue_wx(wqh, xbp, 1, 1);
    issue_wx(wqh, xbp, 2, 2);
    issue_wx(wqh, xbp, 3, 3);
  }
  gptr nwq = wqB + hkv * 256, nx = xB + (size_t)b * (128 * 576);
  for (int item = 0; item < 2; ++item) {
    gptr wqh = (item ? wqB : wqA) + hkv * 256, xbp = (item ? xB : xA) + (size_t)b * (128 * 576);
    gptr Kh = (item ? kB : kA) + kvoff, Vh = (item ? vB : vA) + kvoff;
    f32x16 acc[4] = {};
    bf16x8 fa[10], fb[10];
    auto qstep = [&](bf16x8* Fn, bf16x8* Fc, auto domm, auto dma) {
      const char* base = shm + rs * SLOT;
      ADV(rs);
#pragma unroll
      for (int i = 0; i < 8; ++i) {
        if (domm) acc[i & 3] = MFMA32(Fc[i], Fc[8 + (i >> 2)], acc[i & 3]);
        if (i == 0) {
          Fn[8] = ld128(base + xb0);
          Fn[9] = ld128(base + xb1);
        }
        Fn[i] = tr2(base + wbase + (i >> 2) * 8192 + (i & 3) * 512, 256);
        if (i & 1) dma(i >> 1);
        SB();
      }
      ADV(wsl);
    };
    WAITBAR(9);
    qstep(fa, fb, F_{}, [&](int pc) { piece_wx(wqh, xbp, 4, wsl, pc); });
    for (int c = 0; c < 12; c += 2) {
      WAITBAR(9);
      qstep(fb, fa, T_{}, [&](int pc) { piece_wx(wqh, xbp, c + 5, wsl, pc); });
      WAITBAR(9);
      qstep(fa, fb, T_{}, [&](int pc) { piece_wx(wqh, xbp, c + 6, wsl, pc); });
    }
    WAITBAR(9);
    qstep(fb, fa, T_{}, [&](int pc) { piece_wx(wqh, xbp, 17, wsl, pc); });
    for (int c = 0; c < 4; c += 2) {
      WAITBAR(9);
      qstep(fa, fb, T_{}, [&](int pc) { piece_kv(Kh, Vh, c, wsl, pc); });
      WAITBAR(9);
      qstep(fb, fa, T_{}, [&](int pc) { piece_kv(Kh, Vh, c + 1, wsl, pc); });
    }
    WAITBAR(9);
#pragma unroll
    for (int i = 0; i < 8; ++i) {
      acc[i & 3] = MFMA32(fb[i], fb[8 + (i >> 2)], acc[i & 3]);
      if (i & 1) piece_kv(Kh, Vh, 4, wsl, i >> 1);
      SB();
    }
    ADV(wsl);
    bf16x8 qf[4][2];
#pragma unroll
    for (int cc = 0; cc < 4; ++cc) {
      qf[cc][0] = pack8(acc[cc], 0, QSCALE);
      qf[cc][1] = pack8(acc[cc], 8, QSCALE);
    }

    f32x16 o[4] = {};
    float m, l = 0.f;
    f32x16 negm = {};
    f32x16 A0, A1, B0, B1;
    {
      const char* kp0 = shm + rs * SLOT + kb0;
      const char* kp1 = shm + rs * SLOT + kb1;
      bf16x8 kf[16];
#pragma unroll
      for (int i = 0; i < 16; ++i) kf[i] = ld128(((i & 2) ? kp1 : kp0) + (i & 1) * 8192 + (i >> 2) * 512);
#pragma unroll
      for (int i = 0; i < 16; ++i) {
        if (i & 1) A1 = MFMA32(kf[i], qf[i >> 2][(i >> 1) & 1], i < 2 ? negm : A1);
        else A0 = MFMA32(kf[i], qf[i >> 2][(i >> 1) & 1], i < 2 ? negm : A0);
      }
      float mx = fmaxf(A0[0], A1[0]);
#pragma unroll
      for (int r = 1; r < 16; ++r) mx = fmaxf(fmaxf(mx, A0[r]), A1[r]);
      m = fmaxf(mx, swap32(mx));
#pragma unroll
      for (int r = 0; r < 16; ++r) {
        A0[r] -= m;
        A1[r] -= m;
        negm[r] = -m;
      }
      SB();
    }
    auto astep = [&](f32x16& c0, f32x16& c1, f32x16& n0, f32x16& n1, auto doqk, auto dma) {
      const char* vbp = shm + rs * SLOT + vb;
      ADV(rs);
      const char* kp0 = shm + rs * SLOT + kb0;
      const char* kp1 = shm + rs * SLOT + kb1;
      bf16x8 kf[16], vf[16];
      u32x4 pw[4];
      if (doqk) {
#pragma unroll
        for (int i = 0; i < 4; ++i) kf[i] = ld128(((i & 2) ? kp1 : kp0) + (i & 1) * 8192 + (i >> 2) * 512);
      }
#pragma unroll
      for (int r = 0; r < 4; ++r) c0[r] = __builtin_amdgcn_exp2f(c0[r]);
      SB();
#pragma unroll
      for (int i = 0; i < 16; ++i) {
        if (doqk) {
          if (i & 1) n1 = MFMA32(kf[i], qf[i >> 2][(i >> 1) & 1], i < 2 ? negm : n1);
          else n0 = MFMA32(kf[i], qf[i >> 2][(i >> 1) & 1], i < 2 ? negm : n0);
          if (i < 12) kf[i + 4] = ld128((((i + 4) & 2) ? kp1 : kp0) + (i & 1) * 8192 + ((i + 4) >> 2) * 512);
        }
        if (i < 6) {
          c0[4 + 2 * i] = __builtin_amdgcn_exp2f(c0[4 + 2 * i]);
          c0[5 + 2 * i] = __builtin_amdgcn_exp2f(c0[5 + 2 * i]);
        } else if (i < 14) {
          c1[i - 6] = __builtin_amdgcn_exp2f(c1[i - 6]);
        }
        if (i < 8) pw[i >> 2][i & 3] = cvtpk(c0[2 * i], c0[2 * i + 1]);
        else if (i >= 9 && (i & 1)) pw[2][(i - 9) >> 1] = cvtpk(c1[i - 9], c1[i - 8]);
        if (i >= 12) vf[i - 12] = tr2(vbp + (i - 12) * 512, 2048);
        SB();
      }
      float s0 = c0[0], s1 = c1[0], mx = 0.f;
#pragma unroll
      for (int i = 0; i < 16; ++i) {
        o[i & 3] = MFMA32(vf[i], __builtin_bit_cast(bf16x8, pw[i >> 2]), o[i & 3]);
        if (i < 12) vf[i + 4] = tr2(vbp + ((i + 4) >> 2) * 4096 + (i & 3) * 512, 2048);
        if (i < 8) c1[8 + i] = __builtin_amdgcn_exp2f(c1[8 + i]);
        if (i >= 2 && i <= 8 && !(i & 1)) pw[3][(i - 2) >> 1] = cvtpk(c1[6 + i], c1[7 + i]);
        if (i > 0) {
          asm volatile("v_add_f32_e32 %0, %0, %1" : "+v"(s0) : "v"(c0[i]));
          asm volatile("v_add_f32_e32 %0, %0, %1" : "+v"(s1) : "v"(c1[i]));
        }
        if (doqk) {
          if (i == 1) mx = fmaxf(n0[0], n1[0]);
          if (i >= 1) mx = fmaxf(fmaxf(mx, n0[i]), n1[i]);
        }
        if ((i & 3) == 1) dma(i >> 2);
        SB();
      }
      ADV(wsl);
      l += s0 + s1;
      if (doqk) {
        if (__any(mx > THR)) {
          float rm = fmaxf(mx, swap32(mx));
          float delta = fmaxf(rm, 0.f);
          float alpha = __builtin_amdgcn_exp2f(-delta);
          m += delta;
          l *= alpha;
#pragma unroll
          for (int r = 0; r < 16; ++r) {
            n0[r] -= delta;
            n1[r] -= delta;
            negm[r] = -m;
          }
#pragma unroll
          for (int cc = 0; cc < 4; ++cc)
#pragma unroll
            for (int r = 0; r < 16; ++r) o[cc][r] *= alpha;
        }
      }
    };
    WAITBAR(12);
    wsl = wsl == 0 ? 4 : wsl - 1;
    astep(A0, A1, B0, B1, T_{}, [&](int) {});
    for (int j = 1; j < 11; j += 2) {
      WAITBAR(8);
      astep(B0, B1, A0, A1, T_{}, [&](int pc) { piece_kv(Kh, Vh, j + 4, wsl, pc); });
      WAITBAR(8);
      astep(A0, A1, B0, B1, T_{}, [&](int pc) { piece_kv(Kh, Vh, j + 5, wsl, pc); });
    }
    WAITBAR(8);
    astep(B0, B1, A0, A1, T_{}, [&](int pc) { piece_kv(Kh, Vh, 15, wsl, pc); });
    WAITBAR(8);
    astep(A0, A1, B0, B1, T_{}, [&](int pc) { if (item == 0) piece_wx(nwq, nx, 0, wsl, pc); });
    WAITBAR(4);
    astep(B0, B1, A0, A1, T_{}, [&](int pc) { if (item == 0) piece_wx(nwq, nx, 1, wsl, pc); });
    WAITBAR(0);
    astep(A0, A1, B0, B1, T_{}, [&](int pc) { if (item == 0) piece_wx(nwq, nx, 2, wsl, pc); });
    WAITBAR(0);
    astep(B0, B1, A0, A1, F_{}, [&](int pc) { if (item == 0) piece_wx(nwq, nx, 3, wsl, pc); });
    l += swap32(l);
    const float inv = 1.0f / l;
    unsigned short* dst = ows + ((((size_t)(item * 16 + b) * 4 + tb) * 32 + (2 * hkv + g)) * 8) * 512 + lane * 8;
#pragma unroll
    for (int cc = 0; cc < 4; ++cc) {
      *reinterpret_cast<bf16x8*>(dst + (cc * 2 + 0) * 512) = pack8(o[cc], 0, inv);
      *reinterpret_cast<bf16x8*>(dst + (cc * 2 + 1) * 512) = pack8(o[cc], 8, inv);
    }
  }
}

constexpr int ST2 = 26624;
#define MFMA16(a, b, c) __builtin_amdgcn_mfma_f32_16x16x32_bf16(a, b, c, 0, 0, 0)
__global__ __launch_bounds__(512, 2) void oproj_kernel(gptr ows, gptr woA, gptr woB, float* __restrict__ part) {
  extern __shared__ __attribute__((aligned(16))) char shm[];
  const int tid = threadIdx.x, lane = tid & 63;
  const int w = __builtin_amdgcn_readfirstlane(tid >> 6);
  const int tg = w >> 1, ch = w & 1;
  const int ii = blockIdx.x, xcd = ii & 7, jj = ii >> 3;
  const int prob = xcd >> 2, kq = xcd & 3, tbk = jj >> 1, cH = jj & 1;
  gptr wog = (prob ? woB : woA) + (size_t)(kq * 1024) * 576 + cH * 288;
  gptr og = ows + ((size_t)(prob * 64 + tbk * 4 + tg) * 128 + kq * 32) * 1024 + ch * 512;
  const unsigned ol = lane * 16;
  unsigned goff[3];
#pragma unroll
  for (int pc = 0; pc < 3; ++pc) {
    int L = (pc < 2 ? 2 * w + pc : 16 + (w & 1)) * 64 + lane, row = L / 36, sg = L % 36;
    goff[pc] = row * 1152 + (sg ^ (((row >> 2) & 1) << 1)) * 16;
  }
  const unsigned l0 = ldsaddr(shm);
  auto piece = [&](int st, int slot, int pc) {
    unsigned d = l0 + slot * ST2;
    if (pc == 0) glds(ol, og + (size_t)st * 1024, d + 18432 + w * 1024);
    else if (pc < 3) glds(goff[pc - 1], wog + (size_t)st * (32 * 576), d + (2 * w + pc - 1) * 1024);
    else if (w < 2) glds(goff[2], wog + (size_t)st * (32 * 576), d + (16 + w) * 1024);
  };
  const int gq = lane >> 4, g1 = gq & 1, q4 = (lane & 15) >> 2, p4 = lane & 3;
  const int abase = (16 * (gq >> 1) + 4 * g1 + q4) * 576 + ch * 288 + p4 * 8;
  const int a0 = abase + (ch ? -32 : 32) * g1, a1 = abase + (ch ? 32 : -32) * g1;
  const int ob = 18432 + tg * 2048 + (gq >> 1) * 1024 + (g1 * 32 + (lane & 15)) * 16;
  f32x4 acc[2][9] = {};
  bf16x8 fa[11], fb[11];
  int rs = 0;
  auto step = [&](bf16x8* Fn, bf16x8* Fc, auto domm, auto dma) {
    const char* s = shm + rs * ST2;
#pragma unroll
    for (int cb = 0; cb < 9; ++cb) {
      if (domm) {
        acc[0][cb] = MFMA16(Fc[cb], Fc[9], acc[0][cb]);
        acc[1][cb] = MFMA16(Fc[cb], Fc[10], acc[1][cb]);
      }
      if (cb == 0) {
        Fn[9] = ld128(s + ob);
        Fn[10] = ld128(s + ob + 256);
      }
      Fn[cb] = tr2(s + ((cb & 1) ? a1 : a0) + cb * 32, 4608);
      if (cb & 1) dma(cb >> 1);
      SB();
    }
    rs = rs == 5 ? 0 : rs + 1;
  };
  struct T_ { constexpr operator bool() const { return true; } };
  struct F_ { constexpr operator bool() const { return false; } };
#define WB2(n4, n3) do { if (w < 2) WAITBAR(n4); else WAITBAR(n3); } while (0)
  for (int st = 0; st < 5; ++st)
#pragma unroll
    for (int pc = 0; pc < 4; ++pc) piece(st, st, pc);
  WB2(16, 12);
  step(fa, fb, F_{}, [&](int pc) { piece(5, 5, pc); });
  for (int n = 1; n < 27; n += 2) {
    WB2(16, 12);
    step(fb, fa, T_{}, [&](int pc) { piece(n + 5, rs == 0 ? 5 : rs - 1, pc); });
    WB2(16, 12);
    step(fa, fb, T_{}, [&](int pc) { piece(n + 6, rs == 0 ? 5 : rs - 1, pc); });
  }
  WB2(16, 12);
  step(fb, fa, T_{}, [&](int) {});
  WB2(12, 9);
  step(fa, fb, T_{}, [&](int) {});
  WB2(8, 6);
  step(fb, fa, T_{}, [&](int) {});
  WB2(4, 3);
  step(fa, fb, T_{}, [&](int) {});
  WAITBAR(0);
  step(fb, fa, T_{}, [&](int) {});
#pragma unroll
  for (int cb = 0; cb < 9; ++cb) {
    acc[0][cb] = MFMA16(fb[cb], fb[9], acc[0][cb]);
    acc[1][cb] = MFMA16(fb[cb], fb[10], acc[1][cb]);
  }
  f32x4* dst = reinterpret_cast<f32x4*>(part) + ((size_t)(((kq * 2 + prob) * 16 + tbk) * 2 + cH) * 8 + w) * (18 * 64) + lane;
#pragma unroll
  for (int u = 0; u < 2; ++u)
#pragma unroll
    for (int cb = 0; cb < 9; ++cb) dst[(u * 9 + cb) * 64] = acc[u][cb];
}

constexpr int RB = 3;
__global__ __launch_bounds__(256) void reduce_kernel(const float* __restrict__ part, unsigned short* __restrict__ out) {
  const int gid = blockIdx.x * 256 + threadIdx.x, lane = gid & 63, wv = gid >> 6;
  const int bi = wv % (18 / RB), tw = wv / (18 / RB), w = tw & 7, tile = tw >> 3;
  const int cH = tile & 1, tbk = (tile >> 1) & 15, prob = tile >> 5;
  const f32x4* p = reinterpret_cast<const f32x4*>(part) + ((size_t)tw * 18 + bi * RB) * 64 + lane;
  constexpr size_t KQ = (size_t)64 * 8 * 18 * 64;
  f32x4 v[RB][4];
#pragma unroll
  for (int i = 0; i < RB; ++i)
#pragma unroll
    for (int k = 0; k < 4; ++k) v[i][k] = __builtin_nontemporal_load(p + i * 64 + k * KQ);
#pragma unroll
  for (int i = 0; i < RB; ++i) {
    const int blk = bi * RB + i, u = blk / 9, cb = blk % 9;
    f32x4 s = (v[i][0] + v[i][1]) + (v[i][2] + v[i][3]);
    const int tok = tbk * 128 + (w >> 1) * 32 + u * 16 + (lane & 15);
    const int col = prob * 576 + cH * 288 + (w & 1) * 144 + cb * 16 + 4 * (lane >> 4);
    u32x2 r = {cvtpk(s[0], s[1]), cvtpk(s[2], s[3])};
    *reinterpret_cast<u32x2*>(out + (size_t)tok * 1152 + col) = r;
  }
}

extern "C" void kernel_launch(void* const* d_in, const int* in_sizes, int n_in, void* d_out, int out_size, void* d_ws,
                              size_t ws_size, hipStream_t stream) {
  (void)in_sizes; (void)n_in; (void)out_size; (void)ws_size;
  gptr xA = (gptr)d_in[0], kA = (gptr)d_in[1], vA = (gptr)d_in[2], wqA = (gptr)d_in[3], woA = (gptr)d_in[4];
  gptr xB = (gptr)d_in[5], kB = (gptr)d_in[6], vB = (gptr)d_in[7], wqB = (gptr)d_in[8], woB = (gptr)d_in[9];
  unsigned short* ows = (unsigned short*)d_ws;
  float* part = (float*)((char*)d_ws + ((size_t)1 << 25));
  hipFuncSetAttribute((const void*)attn_kernel, hipFuncAttributeMaxDynamicSharedMemorySize, 163840);
  hipFuncSetAttribute((const void*)oproj_kernel, hipFuncAttributeMaxDynamicSharedMemorySize, 163840);
  hipLaunchKernelGGL(attn_kernel, dim3(256), dim3(512), 163840, stream, xA, kA, vA, wqA, xB, kB, vB, wqB, ows);
  hipLaunchKernelGGL(oproj_kernel, dim3(256), dim3(512), 6 * ST2, stream, (gptr)ows, woA, woB, part);
  hipLaunchKernelGGL(reduce_kernel, dim3(2304 / RB), dim3(256), 0, stream, (const float*)part, (unsigned short*)d_out);
}
```

```cpp
#include <hip/hip_runtime.h>
#include <hip/hip_bf16.h>
#include <stdint.h>

typedef __attribute__((ext_vector_type(8))) short bf16x8;
typedef __attribute__((ext_vector_type(4))) short s16x4;
typedef __attribute__((ext_vector_type(16))) float f32x16;
typedef __attribute__((ext_vector_type(4))) float f32x4;
typedef __attribute__((ext_vector_type(2))) float f32x2;
typedef __attribute__((ext_vector_type(2))) __bf16 bf16x2;
typedef __attribute__((ext_vector_type(2))) _Float16 f16x2;
typedef __attribute__((ext_vector_type(4))) unsigned u32x4;
typedef __attribute__((ext_vector_type(2))) unsigned u32x2;
typedef __attribute__((address_space(3))) s16x4* lds_s16x4_p;
typedef __attribute__((address_space(3))) void* lds_void_p;
typedef const unsigned short* gptr;

#define DEV __device__ __forceinline__

DEV unsigned cvtpk(float lo, float hi) {
  f32x2 v = {lo, hi};
  bf16x2 r = __builtin_convertvector(v, bf16x2);
  return __builtin_bit_cast(unsigned, r);
}
DEV unsigned cvtpkh(float lo, float hi) {
  f32x2 v = {lo, hi};
  f16x2 r = __builtin_convertvector(v, f16x2);
  return __builtin_bit_cast(unsigned, r);
}
DEV bf16x8 pack8(const f32x16& a, int base, float sc) {
  u32x4 w = {cvtpk(a[base + 0] * sc, a[base + 1] * sc), cvtpk(a[base + 2] * sc, a[base + 3] * sc),
             cvtpk(a[base + 4] * sc, a[base + 5] * sc), cvtpk(a[base + 6] * sc, a[base + 7] * sc)};
  return __builtin_bit_cast(bf16x8, w);
}
DEV bf16x8 pack8n(const f32x16& a, int base) {
  u32x4 w = {cvtpk(a[base + 0], a[base + 1]), cvtpk(a[base + 2], a[base + 3]),
             cvtpk(a[base + 4], a[base + 5]), cvtpk(a[base + 6], a[base + 7])};
  return __builtin_bit_cast(bf16x8, w);
}
DEV void glds(unsigned voff, const void* sbase, unsigned lds) {
  unsigned keep;
  asm volatile("s_mov_b32 %0, m0\n\ts_mov_b32 m0, %3\n\ts_nop 0\n\tglobal_load_lds_dwordx4 %1, %2\n\ts_mov_b32 m0, %0"
               : "=&s"(keep) : "v"(voff), "s"(sbase), "s"(lds) : "memory");
}
DEV void glds_nt(unsigned voff, const void* sbase, unsigned lds) {
  unsigned keep;
  asm volatile("s_mov_b32 %0, m0\n\ts_mov_b32 m0, %3\n\ts_nop 0\n\tglobal_load_lds_dwordx4 %1, %2 nt\n\ts_mov_b32 m0, %0"
               : "=&s"(keep) : "v"(voff), "s"(sbase), "s"(lds) : "memory");
}
DEV void touch(unsigned voff, const void* sbase, unsigned lds) {
  unsigned keep;
  asm volatile("s_mov_b32 %0, m0\n\ts_mov_b32 m0, %3\n\ts_nop 0\n\tglobal_load_lds_dword %1, %2\n\ts_mov_b32 m0, %0"
               : "=&s"(keep) : "v"(voff), "s"(sbase), "s"(lds) : "memory");
}
DEV unsigned ldsaddr(const char* p) { return (unsigned)(size_t)(lds_void_p)p; }
#define SB() __builtin_amdgcn_sched_barrier(0)
DEV s16x4 trrd(const char* l) { return __builtin_amdgcn_ds_read_tr16_b64_v4i16((lds_s16x4_p)l); }
DEV bf16x8 tr2(const char* l, int d) {
  s16x4 a = trrd(l), b = trrd(l + d);
  return __builtin_shufflevector(a, b, 0, 1, 2, 3, 4, 5, 6, 7);
}
DEV bf16x8 ld128(const char* l) { return *reinterpret_cast<const bf16x8*>(l); }
DEV float swap32(float x) {
  auto rr = __builtin_amdgcn_permlane32_swap(__float_as_uint(x), __float_as_uint(x), false, false);
  return __uint_as_float((threadIdx.x & 32) ? rr[0] : rr[1]);
}
#define WAITBAR(n)                                                          \
  do {                                                                      \
    __builtin_amdgcn_s_waitcnt(0xC07F);                      \
    asm volatile("s_waitcnt vmcnt(" #n ")\n\ts_barrier" ::: "memory");     \
  } while (0)

constexpr int SLOT = 32768;
constexpr float QSCALE = 0.088388347648318447f * 1.4426950408889634f;
constexpr float THR = 8.0f;

#define MFMA32(a, b, c) __builtin_amdgcn_mfma_f32_32x32x16_bf16(a, b, c, 0, 0, 0)
__global__ __launch_bounds__(512, 2) void attn_kernel(gptr xA, gptr kA, gptr vA, gptr wqA, gptr xB, gptr kB, gptr vB,
                                                      gptr wqB, gptr woA, gptr woB, unsigned short* __restrict__ ows) {
  extern __shared__ __attribute__((aligned(16))) char shm[];
  const int tid = threadIdx.x, lane = tid & 63;
  const int w = __builtin_amdgcn_readfirstlane(tid >> 6);
  const int ii = blockIdx.x, xcd = ii & 7, jj = ii >> 3;
  const int hkv = 2 * xcd + (jj & 1), b = jj >> 1;
  const int g = w >> 2, tb = w & 3;
  const int h = lane >> 5, l31 = lane & 31;
  const size_t kvoff = (size_t)(b * 16 + hkv) * (1024 * 128);

  const int sub = lane >> 5, row7 = (lane & 31) >> 2, slot4 = lane & 3;
  const unsigned kg = 2 * ((8 * w + row7) * 128 + sub * 32 + ((slot4 ^ ((2 * w + (row7 >> 2)) & 3)) * 8));
  const unsigned vg = 2 * ((8 * w + row7) * 128 + sub * 32 + slot4 * 8);
  const unsigned wg = 2 * ((8 * (w >> 1) + row7) * 4096 + ((2 * w) & 3) * 64 + sub * 32 + slot4 * 8);
  const unsigned xg = 2 * ((16 * w + (lane >> 2)) * 576 + ((slot4 ^ ((lane >> 4) & 3)) * 8));
  const unsigned l0 = ldsaddr(shm);

  auto piece_kv = [&](gptr Kh, gptr Vh, int t, int slot, int pc) {
    unsigned dst = l0 + slot * SLOT + w * 2048;
    if (pc == 0) glds_nt(kg, Kh + t * 8192, dst);
    if (pc == 1) glds_nt(kg, Kh + t * 8192 + 64, dst + 1024);
    if (pc == 2) glds_nt(vg, Vh + t * 8192, dst + 16384);
    if (pc == 3) glds_nt(vg, Vh + t * 8192 + 64, dst + 16384 + 1024);
  };
  auto piece_wx = [&](gptr wqh, gptr xbp, int c, int slot, int pc) {
    unsigned base = l0 + slot * SLOT;
    gptr ws_ = wqh + (size_t)c * (32 * 4096);
    if (pc == 0) glds(wg, ws_, base + w * 2048);
    if (pc == 1) glds(wg, ws_ + 64, base + w * 2048 + 1024);
    if (pc == 2) glds(xg, xbp + c * 32, base + 16384 + w * 1024);
  };
  auto issue_wx = [&](gptr wqh, gptr xbp, int c, int slot) {
    piece_wx(wqh, xbp, c, slot, 0);
    piece_wx(wqh, xbp, c, slot, 1);
    piece_wx(wqh, xbp, c, slot, 2);
  };

  const int q4 = (lane & 15) >> 2, p4 = lane & 3, blk = (lane >> 4) & 1;
  const int p4s = ((p4 & 1) << 1) | (p4 >> 1);
  const int wbase = h * 4096 + g * 2048 + q4 * 64 + blk * 32 + p4s * 8;
  const int xrow = 32 * tb + l31;
  const int xb0 = 16384 + 512 * (xrow >> 3) + 64 * (xrow & 7) + 16 * ((0 + h) ^ ((xrow >> 2) & 3));
  const int xb1 = 16384 + 512 * (xrow >> 3) + 64 * (xrow & 7) + 16 * ((2 + h) ^ ((xrow >> 2) & 3));
  const int kb0 = 2048 * (l31 >> 3) + 64 * (l31 & 7) + 16 * ((0 + h) ^ ((l31 >> 2) & 3));
  const int kb1 = 2048 * (l31 >> 3) + 64 * (l31 & 7) + 16 * ((2 + h) ^ ((l31 >> 2) & 3));
  const int vb = 16384 + h * 256 + q4 * 64 + blk * 32 + p4 * 8;

  int rs = 0, wsl = 4;
#define ADV(x) x = (x == 4 ? 0 : x + 1)
  struct T_ { constexpr operator bool() const { return true; } };
  struct F_ { constexpr operator bool() const { return false; } };
  {
    gptr wqh = wqA + hkv * 256, xbp = xA + (size_t)b * (128 * 576);
    issue_wx(wqh, xbp, 0, 0);
    issue_wx(wqh, xbp, 1, 1);
    issue_wx(wqh, xbp, 2, 2);
    issue_wx(wqh, xbp, 3, 3);
  }
  gptr nwq = wqB + hkv * 256, nx = xB + (size_t)b * (128 * 576);
  const char* wosrc = (const char*)((xcd >> 2) ? woB : woA) + (size_t)(xcd & 3) * (1024 * 1152) + (w * 32 + jj) * 4096;
  for (int item = 0; item < 2; ++item) {
    gptr wqh = item ? nwq : wqA + hkv * 256, xbp = item ? nx : xA + (size_t)b * (128 * 576);
    gptr Kh = (item ? kB : kA) + kvoff, Vh = (item ? vB : vA) + kvoff;
    f32x16 acc[4] = {};
    bf16x8 fa[10], fb[10];
    auto qstep = [&](bf16x8* Fn, bf16x8* Fc, auto domm, auto dma) {
      const char* base = shm + rs * SLOT;
      ADV(rs);
#pragma unroll
      for (int i = 0; i < 8; ++i) {
        if (domm) acc[i & 3] = MFMA32(Fc[i], Fc[8 + (i >> 2)], acc[i & 3]);
        if (i == 0) {
          Fn[8] = ld128(base + xb0);
          Fn[9] = ld128(base + xb1);
        }
        Fn[i] = tr2(base + wbase + (i >> 2) * 8192 + (i & 3) * 512, 256);
        if (i & 1) dma(i >> 1);
        SB();
      }
      ADV(wsl);
    };
    WAITBAR(9);
    qstep(fa, fb, F_{}, [&](int pc) { piece_wx(wqh, xbp, 4, wsl, pc); });
    for (int c = 0; c < 12; c += 2) {
      WAITBAR(9);
      qstep(fb, fa, T_{}, [&](int pc) { piece_wx(wqh, xbp, c + 5, wsl, pc); });
      WAITBAR(9);
      qstep(fa, fb, T_{}, [&](int pc) { piece_wx(wqh, xbp, c + 6, wsl, pc); });
    }
    WAITBAR(9);
    qstep(fb, fa, T_{}, [&](int pc) { piece_wx(wqh, xbp, 17, wsl, pc); });
    if (item == 0) {
#pragma unroll
      for (int r = 0; r < 3; ++r) {
        const int t = r * 256 + jj * 8 + w;
        if (t < 720) {
          const char* src = t < 144 ? (const char*)nwq + (t * 32768 - (jj & 1) * 512)
                                    : (const char*)nx + ((t - 144) * 4096 - b * 147456);
          touch(t < 144 ? (lane >> 4) * 8192 + (lane & 15) * 64 : lane * 64, src,
                l0 + (wsl == 0 ? 4 : wsl - 1) * SLOT + 24576 + w * 1024 + r * 256);
        }
      }
    }
    for (int c = 0; c < 4; c += 2) {
      WAITBAR(12);
      qstep(fa, fb, T_{}, [&](int pc) { piece_kv(Kh, Vh, c, wsl, pc); });
      WAITBAR(12);
      qstep(fb, fa, T_{}, [&](int pc) { piece_kv(Kh, Vh, c + 1, wsl, pc); });
    }
    WAITBAR(9);
#pragma unroll
    for (int i = 0; i < 8; ++i) {
      acc[i & 3] = MFMA32(fb[i], fb[8 + (i >> 2)], acc[i & 3]);
      if (i & 1) piece_kv(Kh, Vh, 4, wsl, i >> 1);
      SB();
    }
    ADV(wsl);
    bf16x8 qf[4][2];
#pragma unroll
    for (int cc = 0; cc < 4; ++cc) {
      qf[cc][0] = pack8(acc[cc], 0, QSCALE);
      qf[cc][1] = pack8(acc[cc], 8, QSCALE);
    }

    f32x16 o[4] = {};
    float m, l = 0.f;
    f32x16 negm = {};
    f32x16 A0, A1, B0, B1;
    {
      const char* kp0 = shm + rs * SLOT + kb0;
      const char* kp1 = shm + rs * SLOT + kb1;
      bf16x8 kf[16];
#pragma unroll
      for (int i = 0; i < 16; ++i) kf[i] = ld128(((i & 2) ? kp1 : kp0) + (i & 1) * 8192 + (i >> 2) * 512);
#pragma unroll
      for (int i = 0; i < 16; ++i) {
        if (i & 1) A1 = MFMA32(kf[i], qf[i >> 2][(i >> 1) & 1], i < 2 ? negm : A1);
        else A0 = MFMA32(kf[i], qf[i >> 2][(i >> 1) & 1], i < 2 ? negm : A0);
      }
      float mx = fmaxf(A0[0], A1[0]);
#pragma unroll
      for (int r = 1; r < 16; ++r) mx = fmaxf(fmaxf(mx, A0[r]), A1[r]);
      m = fmaxf(mx, swap32(mx));
#pragma unroll
      for (int r = 0; r < 16; ++r) {
        A0[r] -= m;
        A1[r] -= m;
        negm[r] = -m;
      }
      SB();
    }
    auto astep = [&](f32x16& c0, f32x16& c1, f32x16& n0, f32x16& n1, auto doqk, auto dma) {
      const char* vbp = shm + rs * SLOT + vb;
      ADV(rs);
      const char* kp0 = shm + rs * SLOT + kb0;
      const char* kp1 = shm + rs * SLOT + kb1;
      bf16x8 kf[16], vf[16];
      u32x4 pw[4];
      if (doqk) {
#pragma unroll
        for (int i = 0; i < 4; ++i) kf[i] = ld128(((i & 2) ? kp1 : kp0) + (i & 1) * 8192 + (i >> 2) * 512);
      }
#pragma unroll
      for (int r = 0; r < 4; ++r) c0[r] = __builtin_amdgcn_exp2f(c0[r]);
      SB();
#pragma unroll
      for (int i = 0; i < 16; ++i) {
        if (doqk) {
          if (i & 1) n1 = MFMA32(kf[i], qf[i >> 2][(i >> 1) & 1], i < 2 ? negm : n1);
          else n0 = MFMA32(kf[i], qf[i >> 2][(i >> 1) & 1], i < 2 ? negm : n0);
          if (i < 12) kf[i + 4] = ld128((((i + 4) & 2) ? kp1 : kp0) + (i & 1) * 8192 + ((i + 4) >> 2) * 512);
        }
        if (i < 6) {
          c0[4 + 2 * i] = __builtin_amdgcn_exp2f(c0[4 + 2 * i]);
          c0[5 + 2 * i] = __builtin_amdgcn_exp2f(c0[5 + 2 * i]);
        } else if (i < 14) {
          c1[2 * i - 12] = __builtin_amdgcn_exp2f(c1[2 * i - 12]);
          c1[2 * i - 11] = __builtin_amdgcn_exp2f(c1[2 * i - 11]);
        }
        pw[i >> 2][i & 3] = i < 8 ? cvtpk(c0[2 * i], c0[2 * i + 1]) : cvtpk(c1[2 * i - 16], c1[2 * i - 15]);
        if (i >= 12) vf[i - 12] = tr2(vbp + (i - 12) * 512, 2048);
        SB();
      }
      float s0 = c0[0], s1 = c1[0], mx = 0.f;
#pragma unroll
      for (int i = 0; i < 16; ++i) {
        o[i & 3] = MFMA32(vf[i], __builtin_bit_cast(bf16x8, pw[i >> 2]), o[i & 3]);
        if (i < 12) vf[i + 4] = tr2(vbp + ((i + 4) >> 2) * 4096 + (i & 3) * 512, 2048);
        if (i > 0) {
          asm volatile("v_add_f32_e32 %0, %0, %1" : "+v"(s0) : "v"(c0[i]));
          asm volatile("v_add_f32_e32 %0, %0, %1" : "+v"(s1) : "v"(c1[i]));
        }
        if (doqk) {
          if (i == 1) mx = fmaxf(n0[0], n1[0]);
          if (i >= 1) mx = fmaxf(fmaxf(mx, n0[i]), n1[i]);
        }
        if ((i & 3) == 1) dma(i >> 2);
        SB();
      }
      ADV(wsl);
      l += s0 + s1;
      if (doqk) {
        if (__any(mx > THR)) {
          float rm = fmaxf(mx, swap32(mx));
          float delta = fmaxf(rm, 0.f);
          float alpha = __builtin_amdgcn_exp2f(-delta);
          m += delta;
          l *= alpha;
#pragma unroll
          for (int r = 0; r < 16; ++r) {
            n0[r] -= delta;
            n1[r] -= delta;
            negm[r] = -m;
          }
#pragma unroll
          for (int cc = 0; cc < 4; ++cc)
#pragma unroll
            for (int r = 0; r < 16; ++r) o[cc][r] *= alpha;
        }
      }
    };
    WAITBAR(12);
    wsl = wsl == 0 ? 4 : wsl - 1;
    astep(A0, A1, B0, B1, T_{}, [&](int) {});
    for (int j = 1; j < 11; j += 2) {
      WAITBAR(8);
      astep(B0, B1, A0, A1, T_{}, [&](int pc) { piece_kv(Kh, Vh, j + 4, wsl, pc); });
      WAITBAR(8);
      astep(A0, A1, B0, B1, T_{}, [&](int pc) { piece_kv(Kh, Vh, j + 5, wsl, pc); });
    }
    WAITBAR(8);
    astep(B0, B1, A0, A1, T_{}, [&](int pc) { piece_kv(Kh, Vh, 15, wsl, pc); });
    WAITBAR(8);
    astep(A0, A1, B0, B1, T_{}, [&](int pc) {
      if (item == 0) piece_wx(nwq, nx, 0, wsl, pc);
      else if (pc == 0 || (pc == 1 && w == 0))
        touch(lane * 64, wosrc + pc * (256 * 4096), l0 + wsl * SLOT + (w * 2 + pc) * 256);
    });
    WAITBAR(4);
    astep(B0, B1, A0, A1, T_{}, [&](int pc) { if (item == 0) piece_wx(nwq, nx, 1, wsl, pc); });
    WAITBAR(0);
    astep(A0, A1, B0, B1, T_{}, [&](int pc) { if (item == 0) piece_wx(nwq, nx, 2, wsl, pc); });
    WAITBAR(0);
    astep(B0, B1, A0, A1, F_{}, [&](int pc) { if (item == 0) piece_wx(nwq, nx, 3, wsl, pc); });
    l += swap32(l);
    const float inv = 1.0f / l;
    unsigned short* dst = ows + ((((size_t)(item * 16 + b) * 4 + tb) * 32 + (2 * hkv + g)) * 8) * 512 + lane * 8;
#pragma unroll
    for (int cc = 0; cc < 4; ++cc) {
      *reinterpret_cast<bf16x8*>(dst + (cc * 2 + 0) * 512) = pack8(o[cc], 0, inv);
      *reinterpret_cast<bf16x8*>(dst + (cc * 2 + 1) * 512) = pack8(o[cc], 8, inv);
    }
  }
  asm volatile("s_waitcnt vmcnt(0)" ::: "memory");
}

constexpr size_t PART_SCALES = (size_t)2048 * 9 * 64 * 4;
constexpr int ST2 = 26624;
#define MFMA16(a, b, c) __builtin_amdgcn_mfma_f32_16x16x32_bf16(a, b, c, 0, 0, 0)
__global__ __launch_bounds__(512, 2) void oproj_kernel(gptr ows, gptr woA, gptr woB, float* __restrict__ part) {
  extern __shared__ __attribute__((aligned(16))) char shm[];
  const int tid = threadIdx.x, lane = tid & 63;
  const int w = __builtin_amdgcn_readfirstlane(tid >> 6);
  const int tg = w >> 1, ch = w & 1;
  const int ii = blockIdx.x, xcd = ii & 7, jj = ii >> 3;
  const int prob = xcd >> 2, kq = xcd & 3, tbk = jj >> 1, cH = jj & 1;
  gptr wog = (prob ? woB : woA) + (size_t)(kq * 1024) * 576 + cH * 288;
  gptr og = ows + ((size_t)(prob * 64 + tbk * 4 + tg) * 128 + kq * 32) * 1024 + ch * 512;
  const unsigned ol = lane * 16;
  unsigned goff[3];
#pragma unroll
  for (int pc = 0; pc < 3; ++pc) {
    int L = (pc < 2 ? 2 * w + pc : 16 + (w & 1)) * 64 + lane, row = L / 36, sg = L % 36;
    goff[pc] = row * 1152 + (sg ^ (((row >> 2) & 1) << 1)) * 16;
  }
  const unsigned l0 = ldsaddr(shm);
  auto piece = [&](int st, int slot, int pc) {
    unsigned d = l0 + slot * ST2;
    if (pc == 0) glds(ol, og + (size_t)st * 1024, d + 18432 + w * 1024);
    else if (pc < 3) glds(goff[pc - 1], wog + (size_t)st * (32 * 576), d + (2 * w + pc - 1) * 1024);
    else if (w < 2) glds(goff[2], wog + (size_t)st * (32 * 576), d + (16 + w) * 1024);
  };
  const int gq = lane >> 4, g1 = gq & 1, q4 = (lane & 15) >> 2, p4 = lane & 3;
  const int abase = (16 * (gq >> 1) + 4 * g1 + q4) * 576 + ch * 288 + p4 * 8;
  const int a0 = abase + (ch ? -32 : 32) * g1, a1 = abase + (ch ? 32 : -32) * g1;
  const int ob = 18432 + tg * 2048 + (gq >> 1) * 1024 + (g1 * 32 + (lane & 15)) * 16;
  f32x4 acc[2][9] = {};
  bf16x8 fa[11], fb[11];
  int rs = 0;
  auto step = [&](bf16x8* Fn, bf16x8* Fc, auto domm, auto dma) {
    const char* s = shm + rs * ST2;
#pragma unroll
    for (int cb = 0; cb < 9; ++cb) {
      if (domm) {
        acc[0][cb] = MFMA16(Fc[cb], Fc[9], acc[0][cb]);
        acc[1][cb] = MFMA16(Fc[cb], Fc[10], acc[1][cb]);
      }
      if (cb == 0) {
        Fn[9] = ld128(s + ob);
        Fn[10] = ld128(s + ob + 256);
      }
      Fn[cb] = tr2(s + ((cb & 1) ? a1 : a0) + cb * 32, 4608);
      if (cb & 1) dma(cb >> 1);
      SB();
    }
    rs = rs == 5 ? 0 : rs + 1;
  };
  struct T_ { constexpr operator bool() const { return true; } };
  struct F_ { constexpr operator bool() const { return false; } };
#define WB2(n4, n3) do { if (w < 2) WAITBAR(n4); else WAITBAR(n3); } while (0)
  for (int st = 0; st < 5; ++st)
#pragma unroll
    for (int pc = 0; pc < 4; ++pc) piece(st, st, pc);
  WB2(16, 12);
  step(fa, fb, F_{}, [&](int pc) { piece(5, 5, pc); });
  for (int n = 1; n < 27; n += 2) {
    WB2(16, 12);
    step(fb, fa, T_{}, [&](int pc) { piece(n + 5, rs == 0 ? 5 : rs - 1, pc); });
    WB2(16, 12);
    step(fa, fb, T_{}, [&](int pc) { piece(n + 6, rs == 0 ? 5 : rs - 1, pc); });
  }
  WB2(16, 12);
  step(fb, fa, T_{}, [&](int) {});
  WB2(12, 9);
  step(fa, fb, T_{}, [&](int) {});
  WB2(8, 6);
  step(fb, fa, T_{}, [&](int) {});
  WB2(4, 3);
  step(fa, fb, T_{}, [&](int) {});
  WAITBAR(0);
  step(fb, fa, T_{}, [&](int) {});
#pragma unroll
  for (int cb = 0; cb < 9; ++cb) {
    acc[0][cb] = MFMA16(fb[cb], fb[9], acc[0][cb]);
    acc[1][cb] = MFMA16(fb[cb], fb[10], acc[1][cb]);
  }
  float m = 0.f;
#pragma unroll
  for (int u = 0; u < 2; ++u)
#pragma unroll
    for (int cb = 0; cb < 9; ++cb)
      m = fmaxf(fmaxf(m, fmaxf(fabsf(acc[u][cb][0]), fabsf(acc[u][cb][1]))),
                fmaxf(fabsf(acc[u][cb][2]), fabsf(acc[u][cb][3])));
  int ex = __builtin_amdgcn_frexp_expf(m);
  ex = ex < -100 ? -100 : ex;
  const float sc = __builtin_amdgcn_ldexpf(1.0f, 15 - ex);
  const size_t tw = (size_t)(((kq * 2 + prob) * 16 + tbk) * 2 + cH) * 8 + w;
  u32x4* dst = reinterpret_cast<u32x4*>(part) + tw * (9 * 64) + lane;
#pragma unroll
  for (int j = 0; j < 9; ++j) {
    const f32x4 a = acc[(2 * j) / 9][(2 * j) % 9], b = acc[(2 * j + 1) / 9][(2 * j + 1) % 9];
    u32x4 r = {cvtpkh(a[0] * sc, a[1] * sc), cvtpkh(a[2] * sc, a[3] * sc), cvtpkh(b[0] * sc, b[1] * sc),
               cvtpkh(b[2] * sc, b[3] * sc)};
    dst[j * 64] = r;
  }
  (part + PART_SCALES)[tw * 64 + lane] = __builtin_amdgcn_ldexpf(1.0f, ex - 15);
}

constexpr int RB = 3, TPB3 = 128;
__global__ __launch_bounds__(TPB3) void reduce_kernel(const float* __restrict__ part, unsigned short* __restrict__ out) {
  const int gid = blockIdx.x * TPB3 + threadIdx.x, lane = gid & 63;
  const int wv = __builtin_amdgcn_readfirstlane(gid >> 6);
  const int bi = wv % (9 / RB), tw = wv / (9 / RB), w = tw & 7, tile = tw >> 3;
  const int cH = tile & 1, tbk = (tile >> 1) & 15, prob = tile >> 5;
  const u32x4* p = reinterpret_cast<const u32x4*>(part) + ((size_t)tw * 9 + bi * RB) * 64 + lane;
  const float* sp = part + PART_SCALES + tw * 64 + lane;
  constexpr size_t KQ = (size_t)512 * 9 * 64, KQS = (size_t)512 * 64;
  u32x4 v[RB][4];
  float inv[4];
#pragma unroll
  for (int i = 0; i < RB; ++i)
#pragma unroll
    for (int k = 0; k < 4; ++k) v[i][k] = __builtin_nontemporal_load(p + i * 64 + k * KQ);
#pragma unroll
  for (int k = 0; k < 4; ++k) inv[k] = sp[k * KQS];
  SB();
#pragma unroll
  for (int i = 0; i < RB; ++i)
#pragma unroll
    for (int h = 0; h < 2; ++h) {
      const int blk = 2 * (bi * RB + i) + h, u = blk / 9, cb = blk % 9;
      float s[4];
#pragma unroll
      for (int e = 0; e < 4; ++e) {
        float t[4];
#pragma unroll
        for (int k = 0; k < 4; ++k) {
          const unsigned raw = v[i][k][2 * h + (e >> 1)];
          const f16x2 x = __builtin_bit_cast(f16x2, raw);
          t[k] = (float)x[e & 1] * inv[k];
        }
        s[e] = (t[0] + t[1]) + (t[2] + t[3]);
      }
      const int tok = tbk * 128 + (w >> 1) * 32 + u * 16 + (lane & 15);
      const int col = prob * 576 + cH * 288 + (w & 1) * 144 + cb * 16 + 4 * (lane >> 4);
      u32x2 r = {cvtpk(s[0], s[1]), cvtpk(s[2], s[3])};
      *reinterpret_cast<u32x2*>(out + (size_t)tok * 1152 + col) = r;
    }
}

extern "C" void kernel_launch(void* const* d_in, const int* in_sizes, int n_in, void* d_out, int out_size, void* d_ws,
                              size_t ws_size, hipStream_t stream) {
  (void)in_sizes; (void)n_in; (void)out_size; (void)ws_size;
  gptr xA = (gptr)d_in[0], kA = (gptr)d_in[1], vA = (gptr)d_in[2], wqA = (gptr)d_in[3], woA = (gptr)d_in[4];
  gptr xB = (gptr)d_in[5], kB = (gptr)d_in[6], vB = (gptr)d_in[7], wqB = (gptr)d_in[8], woB = (gptr)d_in[9];
  unsigned short* ows = (unsigned short*)d_ws;
  float* part = (float*)((char*)d_ws + ((size_t)1 << 25));
  hipFuncSetAttribute((const void*)attn_kernel, hipFuncAttributeMaxDynamicSharedMemorySize, 163840);
  hipFuncSetAttribute((const void*)oproj_kernel, hipFuncAttributeMaxDynamicSharedMemorySize, 163840);
  hipLaunchKernelGGL(attn_kernel, dim3(256), dim3(512), 163840, stream, xA, kA, vA, wqA, xB, kB, vB, wqB, woA, woB, ows);
  hipLaunchKernelGGL(oproj_kernel, dim3(256), dim3(512), 6 * ST2, stream, (gptr)ows, woA, woB, part);
  hipLaunchKernelGGL(reduce_kernel, dim3(512 * (9 / RB) * 64 / TPB3), dim3(TPB3), 0, stream, (const float*)part, (unsigned short*)d_out);
}
```

```cpp
#define START_TOUCH 0
#include <hip/hip_runtime.h>
#include <hip/hip_bf16.h>
#include <stdint.h>

typedef __attribute__((ext_vector_type(8))) short bf16x8;
typedef __attribute__((ext_vector_type(4))) short s16x4;
typedef __attribute__((ext_vector_type(16))) float f32x16;
typedef __attribute__((ext_vector_type(4))) float f32x4;
typedef __attribute__((ext_vector_type(2))) float f32x2;
typedef __attribute__((ext_vector_type(2))) __bf16 bf16x2;
typedef __attribute__((ext_vector_type(2))) _Float16 f16x2;
typedef __attribute__((ext_vector_type(4))) unsigned u32x4;
typedef __attribute__((ext_vector_type(2))) unsigned u32x2;
typedef __attribute__((address_space(3))) s16x4* lds_s16x4_p;
typedef __attribute__((address_space(3))) void* lds_void_p;
typedef const unsigned short* gptr;

#define DEV __device__ __forceinline__

DEV unsigned cvtpk(float lo, float hi) {
  f32x2 v = {lo, hi};
  bf16x2 r = __builtin_convertvector(v, bf16x2);
  return __builtin_bit_cast(unsigned, r);
}
DEV unsigned cvtpkh(float lo, float hi) {
  f32x2 v = {lo, hi};
  f16x2 r = __builtin_convertvector(v, f16x2);
  return __builtin_bit_cast(unsigned, r);
}
DEV bf16x8 pack8(const f32x16& a, int base, float sc) {
  u32x4 w = {cvtpk(a[base + 0] * sc, a[base + 1] * sc), cvtpk(a[base + 2] * sc, a[base + 3] * sc),
             cvtpk(a[base + 4] * sc, a[base + 5] * sc), cvtpk(a[base + 6] * sc, a[base + 7] * sc)};
  return __builtin_bit_cast(bf16x8, w);
}
DEV bf16x8 pack8n(const f32x16& a, int base) {
  u32x4 w = {cvtpk(a[base + 0], a[base + 1]), cvtpk(a[base + 2], a[base + 3]),
             cvtpk(a[base + 4], a[base + 5]), cvtpk(a[base + 6], a[base + 7])};
  return __builtin_bit_cast(bf16x8, w);
}
DEV void glds(unsigned voff, const void* sbase, unsigned lds) {
  unsigned keep;
  asm volatile("s_mov_b32 %0, m0\n\ts_mov_b32 m0, %3\n\ts_nop 0\n\tglobal_load_lds_dwordx4 %1, %2\n\ts_mov_b32 m0, %0"
               : "=&s"(keep) : "v"(voff), "s"(sbase), "s"(lds) : "memory");
}
DEV void glds_nt(unsigned voff, const void* sbase, unsigned lds) {
  unsigned keep;
  asm volatile("s_mov_b32 %0, m0\n\ts_mov_b32 m0, %3\n\ts_nop 0\n\tglobal_load_lds_dwordx4 %1, %2 nt\n\ts_mov_b32 m0, %0"
               : "=&s"(keep) : "v"(voff), "s"(sbase), "s"(lds) : "memory");
}
DEV void touch(unsigned voff, const void* sbase, unsigned lds) {
  unsigned keep;
  asm volatile("s_mov_b32 %0, m0\n\ts_mov_b32 m0, %3\n\ts_nop 0\n\tglobal_load_lds_dword %1, %2\n\ts_mov_b32 m0, %0"
               : "=&s"(keep) : "v"(voff), "s"(sbase), "s"(lds) : "memory");
}
DEV unsigned ldsaddr(const char* p) { return (unsigned)(size_t)(lds_void_p)p; }
#define SB() __builtin_amdgcn_sched_barrier(0)
DEV s16x4 trrd(const char* l) { return __builtin_amdgcn_ds_read_tr16_b64_v4i16((lds_s16x4_p)l); }
DEV bf16x8 tr2(const char* l, int d) {
  s16x4 a = trrd(l), b = trrd(l + d);
  return __builtin_shufflevector(a, b, 0, 1, 2, 3, 4, 5, 6, 7);
}
DEV bf16x8 ld128(const char* l) { return *reinterpret_cast<const bf16x8*>(l); }
DEV float swap32(float x) {
  auto rr = __builtin_amdgcn_permlane32_swap(__float_as_uint(x), __float_as_uint(x), false, false);
  return __uint_as_float((threadIdx.x & 32) ? rr[0] : rr[1]);
}
#define WAITBAR(n)                                                          \
  do {                                                                      \
    __builtin_amdgcn_s_waitcnt(0xC07F);                      \
    asm volatile("s_waitcnt vmcnt(" #n ")\n\ts_barrier" ::: "memory");     \
  } while (0)

constexpr int SLOT = 32768;
constexpr float QSCALE = 0.088388347648318447f * 1.4426950408889634f;
constexpr float THR = 8.0f;

#define MFMA32(a, b, c) __builtin_amdgcn_mfma_f32_32x32x16_bf16(a, b, c, 0, 0, 0)
__global__ __launch_bounds__(512, 2) void attn_kernel(gptr xA, gptr kA, gptr vA, gptr wqA, gptr xB, gptr kB, gptr vB,
                                                      gptr wqB, gptr woA, gptr woB, unsigned short* __restrict__ ows) {
  extern __shared__ __attribute__((aligned(16))) char shm[];
  const int tid = threadIdx.x, lane = tid & 63;
  const int w = __builtin_amdgcn_readfirstlane(tid >> 6);
  const int ii = blockIdx.x, xcd = ii & 7, jj = ii >> 3;
  const int hkv = 2 * xcd + (jj & 1), b = jj >> 1;
  const int g = w >> 2, tb = w & 3;
  const int h = lane >> 5, l31 = lane & 31;
  const size_t kvoff = (size_t)(b * 16 + hkv) * (1024 * 128);

  const int sub = lane >> 5, row7 = (lane & 31) >> 2, slot4 = lane & 3;
  const unsigned kg = 2 * ((8 * w + row7) * 128 + sub * 32 + ((slot4 ^ ((2 * w + (row7 >> 2)) & 3)) * 8));
  const unsigned vg = 2 * ((8 * w + row7) * 128 + sub * 32 + slot4 * 8);
  const unsigned wg = 2 * ((8 * (w >> 1) + row7) * 4096 + ((2 * w) & 3) * 64 + sub * 32 + slot4 * 8);
  const unsigned xg = 2 * ((16 * w + (lane >> 2)) * 576 + ((slot4 ^ ((lane >> 4) & 3)) * 8));
  const unsigned l0 = ldsaddr(shm);

  auto piece_kv = [&](gptr Kh, gptr Vh, int t, int slot, int pc) {
    unsigned dst = l0 + slot * SLOT + w * 2048;
    if (pc == 0) glds_nt(kg, Kh + t * 8192, dst);
    if (pc == 1) glds_nt(kg, Kh + t * 8192 + 64, dst + 1024);
    if (pc == 2) glds_nt(vg, Vh + t * 8192, dst + 16384);
    if (pc == 3) glds_nt(vg, Vh + t * 8192 + 64, dst + 16384 + 1024);
  };
  auto piece_wx = [&](gptr wqh, gptr xbp, int c, int slot, int pc) {
    unsigned base = l0 + slot * SLOT;
    gptr ws_ = wqh + (size_t)c * (32 * 4096);
    if (pc == 0) glds(wg, ws_, base + w * 2048);
    if (pc == 1) glds(wg, ws_ + 64, base + w * 2048 + 1024);
    if (pc == 2) glds(xg, xbp + c * 32, base + 16384 + w * 1024);
  };
  auto issue_wx = [&](gptr wqh, gptr xbp, int c, int slot) {
    piece_wx(wqh, xbp, c, slot, 0);
    piece_wx(wqh, xbp, c, slot, 1);
    piece_wx(wqh, xbp, c, slot, 2);
  };

  const int q4 = (lane & 15) >> 2, p4 = lane & 3, blk = (lane >> 4) & 1;
  const int p4s = ((p4 & 1) << 1) | (p4 >> 1);
  const int wbase = h * 4096 + g * 2048 + q4 * 64 + blk * 32 + p4s * 8;
  const int xrow = 32 * tb + l31;
  const int xb0 = 16384 + 512 * (xrow >> 3) + 64 * (xrow & 7) + 16 * ((0 + h) ^ ((xrow >> 2) & 3));
  const int xb1 = 16384 + 512 * (xrow >> 3) + 64 * (xrow & 7) + 16 * ((2 + h) ^ ((xrow >> 2) & 3));
  const int kb0 = 2048 * (l31 >> 3) + 64 * (l31 & 7) + 16 * ((0 + h) ^ ((l31 >> 2) & 3));
  const int kb1 = 2048 * (l31 >> 3) + 64 * (l31 & 7) + 16 * ((2 + h) ^ ((l31 >> 2) & 3));
  const int vb = 16384 + h * 256 + q4 * 64 + blk * 32 + p4 * 8;

  int rs = 0, wsl = 4;
#define ADV(x) x = (x == 4 ? 0 : x + 1)
  struct T_ { constexpr operator bool() const { return true; } };
  struct F_ { constexpr operator bool() const { return false; } };
  {
    gptr wqh = wqA + hkv * 256, xbp = xA + (size_t)b * (128 * 576);
    issue_wx(wqh, xbp, 0, 0);
    issue_wx(wqh, xbp, 1, 1);
    issue_wx(wqh, xbp, 2, 2);
    issue_wx(wqh, xbp, 3, 3);
  }
  gptr nwq = wqB + hkv * 256, nx = xB + (size_t)b * (128 * 576);
  for (int item = 0; item < 2; ++item) {
    gptr wqh = (item ? wqB : wqA) + hkv * 256, xbp = (item ? xB : xA) + (size_t)b * (128 * 576);
    gptr Kh = (item ? kB : kA) + kvoff, Vh = (item ? vB : vA) + kvoff;
    f32x16 acc[4] = {};
    bf16x8 fa[10], fb[10];
    auto qstep = [&](bf16x8* Fn, bf16x8* Fc, auto domm, auto dma) {
      const char* base = shm + rs * SLOT;
      ADV(rs);
#pragma unroll
      for (int i = 0; i < 8; ++i) {
        if (domm) acc[i & 3] = MFMA32(Fc[i], Fc[8 + (i >> 2)], acc[i & 3]);
        if (i == 0) {
          Fn[8] = ld128(base + xb0);
          Fn[9] = ld128(base + xb1);
        }
        Fn[i] = tr2(base + wbase + (i >> 2) * 8192 + (i & 3) * 512, 256);
        if (i & 1) dma(i >> 1);
        SB();
      }
      ADV(wsl);
    };
    WAITBAR(9);
    qstep(fa, fb, F_{}, [&](int pc) { piece_wx(wqh, xbp, 4, wsl, pc); });
    for (int c = 0; c < 12; c += 2) {
      WAITBAR(9);
      qstep(fb, fa, T_{}, [&](int pc) { piece_wx(wqh, xbp, c + 5, wsl, pc); });
      WAITBAR(9);
      qstep(fa, fb, T_{}, [&](int pc) { piece_wx(wqh, xbp, c + 6, wsl, pc); });
    }
    WAITBAR(9);
    qstep(fb, fa, T_{}, [&](int pc) { piece_wx(wqh, xbp, 17, wsl, pc); });
    for (int c = 0; c < 4; c += 2) {
      WAITBAR(9);
      qstep(fa, fb, T_{}, [&](int pc) { piece_kv(Kh, Vh, c, wsl, pc); });
      WAITBAR(9);
      qstep(fb, fa, T_{}, [&](int pc) { piece_kv(Kh, Vh, c + 1, wsl, pc); });
    }
    WAITBAR(9);
#pragma unroll
    for (int i = 0; i < 8; ++i) {
      acc[i & 3] = MFMA32(fb[i], fb[8 + (i >> 2)], acc[i & 3]);
      if (i & 1) piece_kv(Kh, Vh, 4, wsl, i >> 1);
      SB();
    }
    ADV(wsl);
    bf16x8 qf[4][2];
#pragma unroll
    for (int cc = 0; cc < 4; ++cc) {
      qf[cc][0] = pack8(acc[cc], 0, QSCALE);
      qf[cc][1] = pack8(acc[cc], 8, QSCALE);
    }

    f32x16 o[4] = {};
    float m, l = 0.f;
    f32x16 negm = {};
    f32x16 A0, A1, B0, B1;
    {
      const char* kp0 = shm + rs * SLOT + kb0;
      const char* kp1 = shm + rs * SLOT + kb1;
      bf16x8 kf[16];
#pragma unroll
      for (int i = 0; i < 16; ++i) kf[i] = ld128(((i & 2) ? kp1 : kp0) + (i & 1) * 8192 + (i >> 2) * 512);
#pragma unroll
      for (int i = 0; i < 16; ++i) {
        if (i & 1) A1 = MFMA32(kf[i], qf[i >> 2][(i >> 1) & 1], i < 2 ? negm : A1);
        else A0 = MFMA32(kf[i], qf[i >> 2][(i >> 1) & 1], i < 2 ? negm : A0);
      }
      float mx = fmaxf(A0[0], A1[0]);
#pragma unroll
      for (int r = 1; r < 16; ++r) mx = fmaxf(fmaxf(mx, A0[r]), A1[r]);
      m = fmaxf(mx, swap32(mx));
#pragma unroll
      for (int r = 0; r < 16; ++r) {
        A0[r] -= m;
        A1[r] -= m;
        negm[r] = -m;
      }
      SB();
    }
    auto astep = [&](f32x16& c0, f32x16& c1, f32x16& n0, f32x16& n1, auto doqk, auto dma) {
      const char* vbp = shm + rs * SLOT + vb;
      ADV(rs);
      const char* kp0 = shm + rs * SLOT + kb0;
      const char* kp1 = shm + rs * SLOT + kb1;
      bf16x8 kf[16], vf[16];
      u32x4 pw[4];
      if (doqk) {
#pragma unroll
        for (int i = 0; i < 4; ++i) kf[i] = ld128(((i & 2) ? kp1 : kp0) + (i & 1) * 8192 + (i >> 2) * 512);
      }
#pragma unroll
      for (int r = 0; r < 4; ++r) c0[r] = __builtin_amdgcn_exp2f(c0[r]);
      SB();
#pragma unroll
      for (int i = 0; i < 16; ++i) {
        if (doqk) {
          if (i & 1) n1 = MFMA32(kf[i], qf[i >> 2][(i >> 1) & 1], i < 2 ? negm : n1);
          else n0 = MFMA32(kf[i], qf[i >> 2][(i >> 1) & 1], i < 2 ? negm : n0);
          if (i < 12) kf[i + 4] = ld128((((i + 4) & 2) ? kp1 : kp0) + (i & 1) * 8192 + ((i + 4) >> 2) * 512);
        }
        if (i < 6) {
          c0[4 + 2 * i] = __builtin_amdgcn_exp2f(c0[4 + 2 * i]);
          c0[5 + 2 * i] = __builtin_amdgcn_exp2f(c0[5 + 2 * i]);
        } else if (i < 14) {
          c1[2 * i - 12] = __builtin_amdgcn_exp2f(c1[2 * i - 12]);
          c1[2 * i - 11] = __builtin_amdgcn_exp2f(c1[2 * i - 11]);
        }
        pw[i >> 2][i & 3] = i < 8 ? cvtpk(c0[2 * i], c0[2 * i + 1]) : cvtpk(c1[2 * i - 16], c1[2 * i - 15]);
        if (i >= 12) vf[i - 12] = tr2(vbp + (i - 12) * 512, 2048);
        SB();
      }
      float s0 = c0[0], s1 = c1[0], mx = 0.f;
#pragma unroll
      for (int i = 0; i < 16; ++i) {
        o[i & 3] = MFMA32(vf[i], __builtin_bit_cast(bf16x8, pw[i >> 2]), o[i & 3]);
        if (i < 12) vf[i + 4] = tr2(vbp + ((i + 4) >> 2) * 4096 + (i & 3) * 512, 2048);
        if (i > 0) {
          asm volatile("v_add_f32_e32 %0, %0, %1" : "+v"(s0) : "v"(c0[i]));
          asm volatile("v_add_f32_e32 %0, %0, %1" : "+v"(s1) : "v"(c1[i]));
        }
        if (doqk) {
          if (i == 1) mx = fmaxf(n0[0], n1[0]);
          if (i >= 1) mx = fmaxf(fmaxf(mx, n0[i]), n1[i]);
        }
        if ((i & 3) == 1) dma(i >> 2);
        SB();
      }
      ADV(wsl);
      l += s0 + s1;
      if (doqk) {
        if (__any(mx > THR)) {
          float rm = fmaxf(mx, swap32(mx));
          float delta = fmaxf(rm, 0.f);
          float alpha = __builtin_amdgcn_exp2f(-delta);
          m += delta;
          l *= alpha;
#pragma unroll
          for (int r = 0; r < 16; ++r) {
            n0[r] -= delta;
            n1[r] -= delta;
            negm[r] = -m;
          }
#pragma unroll
          for (int cc = 0; cc < 4; ++cc)
#pragma unroll
            for (int r = 0; r < 16; ++r) o[cc][r] *= alpha;
        }
      }
    };
    WAITBAR(12);
    wsl = wsl == 0 ? 4 : wsl - 1;
    astep(A0, A1, B0, B1, T_{}, [&](int) {});
    for (int j = 1; j < 11; j += 2) {
      WAITBAR(8);
      astep(B0, B1, A0, A1, T_{}, [&](int pc) { piece_kv(Kh, Vh, j + 4, wsl, pc); });
      WAITBAR(8);
      astep(A0, A1, B0, B1, T_{}, [&](int pc) { piece_kv(Kh, Vh, j + 5, wsl, pc); });
    }
    WAITBAR(8);
    astep(B0, B1, A0, A1, T_{}, [&](int pc) { piece_kv(Kh, Vh, 15, wsl, pc); });
    WAITBAR(8);
    astep(A0, A1, B0, B1, T_{}, [&](int pc) {
      if (item == 0) piece_wx(nwq, nx, 0, wsl, pc);
      else if (pc == 0 || (pc == 1 && w == 0))
        touch(lane * 64, (const char*)((xcd >> 2) ? woB : woA) + (size_t)(xcd & 3) * (1024 * 1152) + (pc * 256 + w * 32 + jj) * 4096,
              l0 + wsl * SLOT + (w * 2 + pc) * 256);
    });
    WAITBAR(4);
    astep(B0, B1, A0, A1, T_{}, [&](int pc) { if (item == 0) piece_wx(nwq, nx, 1, wsl, pc); });
    WAITBAR(0);
    astep(A0, A1, B0, B1, T_{}, [&](int pc) { if (item == 0) piece_wx(nwq, nx, 2, wsl, pc); });
    WAITBAR(0);
    astep(B0, B1, A0, A1, F_{}, [&](int pc) { if (item == 0) piece_wx(nwq, nx, 3, wsl, pc); });
    l += swap32(l);
    const float inv = 1.0f / l;
    unsigned short* dst = ows + ((((size_t)(item * 16 + b) * 4 + tb) * 32 + (2 * hkv + g)) * 8) * 512 + lane * 8;
#pragma unroll
    for (int cc = 0; cc < 4; ++cc) {
      *reinterpret_cast<bf16x8*>(dst + (cc * 2 + 0) * 512) = pack8(o[cc], 0, inv);
      *reinterpret_cast<bf16x8*>(dst + (cc * 2 + 1) * 512) = pack8(o[cc], 8, inv);
    }
  }
  asm volatile("s_waitcnt vmcnt(0)" ::: "memory");
}

constexpr size_t PART_SCALES = (size_t)2048 * 9 * 64 * 4;
constexpr int ST2 = 26624;
#define MFMA16(a, b, c) __builtin_amdgcn_mfma_f32_16x16x32_bf16(a, b, c, 0, 0, 0)
__global__ __launch_bounds__(512, 2) void oproj_kernel(gptr ows, gptr woA, gptr woB, float* __restrict__ part) {
  extern __shared__ __attribute__((aligned(16))) char shm[];
  const int tid = threadIdx.x, lane = tid & 63;
  const int w = __builtin_amdgcn_readfirstlane(tid >> 6);
  const int tg = w >> 1, ch = w & 1;
  const int ii = blockIdx.x, xcd = ii & 7, jj = ii >> 3;
  const int prob = xcd >> 2, kq = xcd & 3, tbk = jj >> 1, cH = jj & 1;
  gptr wog = (prob ? woB : woA) + (size_t)(kq * 1024) * 576 + cH * 288;
  gptr og = ows + ((size_t)(prob * 64 + tbk * 4 + tg) * 128 + kq * 32) * 1024 + ch * 512;
  const unsigned ol = lane * 16;
  unsigned goff[3];
#pragma unroll
  for (int pc = 0; pc < 3; ++pc) {
    int L = (pc < 2 ? 2 * w + pc : 16 + (w & 1)) * 64 + lane, row = L / 36, sg = L % 36;
    goff[pc] = row * 1152 + (sg ^ (((row >> 2) & 1) << 1)) * 16;
  }
  const unsigned l0 = ldsaddr(shm);
  auto piece = [&](int st, int slot, int pc) {
    unsigned d = l0 + slot * ST2;
    if (pc == 0) glds(ol, og + (size_t)st * 1024, d + 18432 + w * 1024);
    else if (pc < 3) glds(goff[pc - 1], wog + (size_t)st * (32 * 576), d + (2 * w + pc - 1) * 1024);
    else if (w < 2) glds(goff[2], wog + (size_t)st * (32 * 576), d + (16 + w) * 1024);
  };
  const int gq = lane >> 4, g1 = gq & 1, q4 = (lane & 15) >> 2, p4 = lane & 3;
  const int abase = (16 * (gq >> 1) + 4 * g1 + q4) * 576 + ch * 288 + p4 * 8;
  const int a0 = abase + (ch ? -32 : 32) * g1, a1 = abase + (ch ? 32 : -32) * g1;
  const int ob = 18432 + tg * 2048 + (gq >> 1) * 1024 + (g1 * 32 + (lane & 15)) * 16;
  f32x4 acc[2][9] = {};
  bf16x8 fa[11], fb[11];
  int rs = 0;
  auto step = [&](bf16x8* Fn, bf16x8* Fc, auto domm, auto dma) {
    const char* s = shm + rs * ST2;
#pragma unroll
    for (int cb = 0; cb < 9; ++cb) {
      if (domm) {
        acc[0][cb] = MFMA16(Fc[cb], Fc[9], acc[0][cb]);
        acc[1][cb] = MFMA16(Fc[cb], Fc[10], acc[1][cb]);
      }
      if (cb == 0) {
        Fn[9] = ld128(s + ob);
        Fn[10] = ld128(s + ob + 256);
      }
      Fn[cb] = tr2(s + ((cb & 1) ? a1 : a0) + cb * 32, 4608);
      if (cb & 1) dma(cb >> 1);
      SB();
    }
    rs = rs == 5 ? 0 : rs + 1;
  };
  struct T_ { constexpr operator bool() const { return true; } };
  struct F_ { constexpr operator bool() const { return false; } };
#define WB2(n4, n3) do { if (w < 2) WAITBAR(n4); else WAITBAR(n3); } while (0)
  for (int st = 0; st < 5; ++st)
#pragma unroll
    for (int pc = 0; pc < 4; ++pc) piece(st, st, pc);
  WB2(16, 12);
  step(fa, fb, F_{}, [&](int pc) { piece(5, 5, pc); });
  for (int n = 1; n < 27; n += 2) {
    WB2(16, 12);
    step(fb, fa, T_{}, [&](int pc) { piece(n + 5, rs == 0 ? 5 : rs - 1, pc); });
    WB2(16, 12);
    step(fa, fb, T_{}, [&](int pc) { piece(n + 6, rs == 0 ? 5 : rs - 1, pc); });
  }
  WB2(16, 12);
  step(fb, fa, T_{}, [&](int) {});
  WB2(12, 9);
  step(fa, fb, T_{}, [&](int) {});
  WB2(8, 6);
  step(fb, fa, T_{}, [&](int) {});
  WB2(4, 3);
  step(fa, fb, T_{}, [&](int) {});
  WAITBAR(0);
  step(fb, fa, T_{}, [&](int) {});
#pragma unroll
  for (int cb = 0; cb < 9; ++cb) {
    acc[0][cb] = MFMA16(fb[cb], fb[9], acc[0][cb]);
    acc[1][cb] = MFMA16(fb[cb], fb[10], acc[1][cb]);
  }
  float m = 0.f;
#pragma unroll
  for (int u = 0; u < 2; ++u)
#pragma unroll
    for (int cb = 0; cb < 9; ++cb)
      m = fmaxf(fmaxf(m, fmaxf(fabsf(acc[u][cb][0]), fabsf(acc[u][cb][1]))),
                fmaxf(fabsf(acc[u][cb][2]), fabsf(acc[u][cb][3])));
  int ex = __builtin_amdgcn_frexp_expf(m);
  ex = ex < -100 ? -100 : ex;
  const float sc = __builtin_amdgcn_ldexpf(1.0f, 15 - ex);
  const size_t tw = (size_t)(((kq * 2 + prob) * 16 + tbk) * 2 + cH) * 8 + w;
  u32x4* dst = reinterpret_cast<u32x4*>(part) + tw * (9 * 64) + lane;
#pragma unroll
  for (int j = 0; j < 9; ++j) {
    const f32x4 a = acc[(2 * j) / 9][(2 * j) % 9], b = acc[(2 * j + 1) / 9][(2 * j + 1) % 9];
    u32x4 r = {cvtpkh(a[0] * sc, a[1] * sc), cvtpkh(a[2] * sc, a[3] * sc), cvtpkh(b[0] * sc, b[1] * sc),
               cvtpkh(b[2] * sc, b[3] * sc)};
    dst[j * 64] = r;
  }
  (part + PART_SCALES)[tw * 64 + lane] = __builtin_amdgcn_ldexpf(1.0f, ex - 15);
}

constexpr int RB = 3, TPB3 = 128;
__global__ __launch_bounds__(TPB3) void reduce_kernel(const float* __restrict__ part, unsigned short* __restrict__ out) {
  const int gid = blockIdx.x * TPB3 + threadIdx.x, lane = gid & 63;
  const int wv = __builtin_amdgcn_readfirstlane(gid >> 6);
  const int bi = wv % (9 / RB), tw = wv / (9 / RB), w = tw & 7, tile = tw >> 3;
  const int cH = tile & 1, tbk = (tile >> 1) & 15, prob = tile >> 5;
  const u32x4* p = reinterpret_cast<const u32x4*>(part) + ((size_t)tw * 9 + bi * RB) * 64 + lane;
  const float* sp = part + PART_SCALES + tw * 64 + lane;
  constexpr size_t KQ = (size_t)512 * 9 * 64, KQS = (size_t)512 * 64;
  u32x4 v[RB][4];
  float inv[4];
#pragma unroll
  for (int i = 0; i < RB; ++i)
#pragma unroll
    for (int k = 0; k < 4; ++k) v[i][k] = __builtin_nontemporal_load(p + i * 64 + k * KQ);
#pragma unroll
  for (int k = 0; k < 4; ++k) inv[k] = sp[k * KQS];
  SB();
#pragma unroll
  for (int i = 0; i < RB; ++i)
#pragma unroll
    for (int h = 0; h < 2; ++h) {
      const int blk = 2 * (bi * RB + i) + h, u = blk / 9, cb = blk % 9;
      float s[4];
#pragma unroll
      for (int e = 0; e < 4; ++e) {
        float t[4];
#pragma unroll
        for (int k = 0; k < 4; ++k) {
          const unsigned raw = v[i][k][2 * h + (e >> 1)];
          const f16x2 x = __builtin_bit_cast(f16x2, raw);
          t[k] = (float)x[e & 1] * inv[k];
        }
        s[e] = (t[0] + t[1]) + (t[2] + t[3]);
      }
      const int tok = tbk * 128 + (w >> 1) * 32 + u * 16 + (lane & 15);
      const int col = prob * 576 + cH * 288 + (w & 1) * 144 + cb * 16 + 4 * (lane >> 4);
      u32x2 r = {cvtpk(s[0], s[1]), cvtpk(s[2], s[3])};
      *reinterpret_cast<u32x2*>(out + (size_t)tok * 1152 + col) = r;
    }
}

extern "C" void kernel_launch(void* const* d_in, const int* in_sizes, int n_in, void* d_out, int out_size, void* d_ws,
                              size_t ws_size, hipStream_t stream) {
  (void)in_sizes; (void)n_in; (void)out_size; (void)ws_size;
  gptr xA = (gptr)d_in[0], kA = (gptr)d_in[1], vA = (gptr)d_in[2], wqA = (gptr)d_in[3], woA = (gptr)d_in[4];
  gptr xB = (gptr)d_in[5], kB = (gptr)d_in[6], vB = (gptr)d_in[7], wqB = (gptr)d_in[8], woB = (gptr)d_in[9];
  unsigned short* ows = (unsigned short*)d_ws;
  float* part = (float*)((char*)d_ws + ((size_t)1 << 25));
  hipFuncSetAttribute((const void*)attn_kernel, hipFuncAttributeMaxDynamicSharedMemorySize, 163840);
  hipFuncSetAttribute((const void*)oproj_kernel, hipFuncAttributeMaxDynamicSharedMemorySize, 163840);
  hipLaunchKernelGGL(attn_kernel, dim3(256), dim3(512), 163840, stream, xA, kA, vA, wqA, xB, kB, vB, wqB, woA, woB, ows);
  hipLaunchKernelGGL(oproj_kernel, dim3(256), dim3(512), 6 * ST2, stream, (gptr)ows, woA, woB, part);
  hipLaunchKernelGGL(reduce_kernel, dim3(512 * (9 / RB) * 64 / TPB3), dim3(TPB3), 0, stream, (const float*)part, (unsigned short*)d_out);
}
```

```cpp
#include <hip/hip_runtime.h>
#include <hip/hip_bf16.h>
#include <stdint.h>

typedef __attribute__((ext_vector_type(8))) short bf16x8;
typedef __attribute__((ext_vector_type(4))) short s16x4;
typedef __attribute__((ext_vector_type(16))) float f32x16;
typedef __attribute__((ext_vector_type(4))) float f32x4;
typedef __attribute__((ext_vector_type(2))) float f32x2;
typedef __attribute__((ext_vector_type(2))) __bf16 bf16x2;
typedef __attribute__((ext_vector_type(2))) _Float16 f16x2;
typedef __attribute__((ext_vector_type(4))) unsigned u32x4;
typedef __attribute__((ext_vector_type(2))) unsigned u32x2;
typedef __attribute__((address_space(3))) s16x4* lds_s16x4_p;
typedef __attribute__((address_space(3))) void* lds_void_p;
typedef const unsigned short* gptr;

#define DEV __device__ __forceinline__

DEV unsigned cvtpk(float lo, float hi) {
  f32x2 v = {lo, hi};
  bf16x2 r = __builtin_convertvector(v, bf16x2);
  return __builtin_bit_cast(unsigned, r);
}
DEV unsigned cvtpkh(float lo, float hi) {
  f32x2 v = {lo, hi};
  f16x2 r = __builtin_convertvector(v, f16x2);
  return __builtin_bit_cast(unsigned, r);
}
DEV bf16x8 pack8(const f32x16& a, int base, float sc) {
  u32x4 w = {cvtpk(a[base + 0] * sc, a[base + 1] * sc), cvtpk(a[base + 2] * sc, a[base + 3] * sc),
             cvtpk(a[base + 4] * sc, a[base + 5] * sc), cvtpk(a[base + 6] * sc, a[base + 7] * sc)};
  return __builtin_bit_cast(bf16x8, w);
}
DEV bf16x8 pack8n(const f32x16& a, int base) {
  u32x4 w = {cvtpk(a[base + 0], a[base + 1]), cvtpk(a[base + 2], a[base + 3]),
             cvtpk(a[base + 4], a[base + 5]), cvtpk(a[base + 6], a[base + 7])};
  return __builtin_bit_cast(bf16x8, w);
}
DEV void glds(unsigned voff, const void* sbase, unsigned lds) {
  unsigned keep;
  asm volatile("s_mov_b32 %0, m0\n\ts_mov_b32 m0, %3\n\ts_nop 0\n\tglobal_load_lds_dwordx4 %1, %2\n\ts_mov_b32 m0, %0"
               : "=&s"(keep) : "v"(voff), "s"(sbase), "s"(lds) : "memory");
}
DEV void glds_nt(unsigned voff, const void* sbase, unsigned lds) {
  unsigned keep;
  asm volatile("s_mov_b32 %0, m0\n\ts_mov_b32 m0, %3\n\ts_nop 0\n\tglobal_load_lds_dwordx4 %1, %2 nt\n\ts_mov_b32 m0, %0"
               : "=&s"(keep) : "v"(voff), "s"(sbase), "s"(lds) : "memory");
}
DEV void touch(unsigned voff, const void* sbase, unsigned lds) {
  unsigned keep;
  asm volatile("s_mov_b32 %0, m0\n\ts_mov_b32 m0, %3\n\ts_nop 0\n\tglobal_load_lds_dword %1, %2\n\ts_mov_b32 m0, %0"
               : "=&s"(keep) : "v"(voff), "s"(sbase), "s"(lds) : "memory");
}
DEV unsigned ldsaddr(const char* p) { return (unsigned)(size_t)(lds_void_p)p; }
#define SB() __builtin_amdgcn_sched_barrier(0)
DEV s16x4 trrd(const char* l) { return __builtin_amdgcn_ds_read_tr16_b64_v4i16((lds_s16x4_p)l); }
DEV bf16x8 tr2(const char* l, int d) {
  s16x4 a = trrd(l), b = trrd(l + d);
  return __builtin_shufflevector(a, b, 0, 1, 2, 3, 4, 5, 6, 7);
}
DEV bf16x8 ld128(const char* l) { return *reinterpret_cast<const bf16x8*>(l); }
DEV float swap32(float x) {
  auto rr = __builtin_amdgcn_permlane32_swap(__float_as_uint(x), __float_as_uint(x), false, false);
  return __uint_as_float((threadIdx.x & 32) ? rr[0] : rr[1]);
}
#define WAITBAR(n)                                                          \
  do {                                                                      \
    __builtin_amdgcn_s_waitcnt(0xC07F);                      \
    asm volatile("s_waitcnt vmcnt(" #n ")\n\ts_barrier" ::: "memory");     \
  } while (0)

constexpr int SLOT = 32768;
constexpr float QSCALE = 0.088388347648318447f * 1.4426950408889634f;
constexpr float THR = 8.0f;

#define MFMA32(a, b, c) __builtin_amdgcn_mfma_f32_32x32x16_bf16(a, b, c, 0, 0, 0)
__global__ __launch_bounds__(512, 2) void attn_kernel(gptr xA, gptr kA, gptr vA, gptr wqA, gptr xB, gptr kB, gptr vB,
                                                      gptr wqB, gptr woA, gptr woB, unsigned short* __restrict__ ows) {
  extern __shared__ __attribute__((aligned(16))) char shm[];
  const int tid = threadIdx.x, lane = tid & 63;
  const int w = __builtin_amdgcn_readfirstlane(tid >> 6);
  const int ii = blockIdx.x, xcd = ii & 7, jj = ii >> 3;
  const int hkv = 2 * xcd + (jj & 1), b = jj >> 1;
  const int g = w >> 2, tb = w & 3;
  const int h = lane >> 5, l31 = lane & 31;
  const size_t kvoff = (size_t)(b * 16 + hkv) * (1024 * 128);

  const int sub = lane >> 5, row7 = (lane & 31) >> 2, slot4 = lane & 3;
  const unsigned kg = 2 * ((8 * w + row7) * 128 + sub * 32 + ((slot4 ^ ((2 * w + (row7 >> 2)) & 3)) * 8));
  const unsigned vg = 2 * ((8 * w + row7) * 128 + sub * 32 + slot4 * 8);
  const unsigned wg = 2 * ((8 * (w >> 1) + row7) * 4096 + ((2 * w) & 3) * 64 + sub * 32 + slot4 * 8);
  const unsigned xg = 2 * ((16 * w + (lane >> 2)) * 576 + ((slot4 ^ ((lane >> 4) & 3)) * 8));
  const unsigned l0 = ldsaddr(shm);

  auto piece_kv = [&](gptr Kh, gptr Vh, int t, int slot, int pc) {
    unsigned dst = l0 + slot * SLOT + w * 2048;
    if (pc == 0) glds_nt(kg, Kh + t * 8192, dst);
    if (pc == 1) glds_nt(kg, Kh + t * 8192 + 64, dst + 1024);
    if (pc == 2) glds_nt(vg, Vh + t * 8192, dst + 16384);
    if (pc == 3) glds_nt(vg, Vh + t * 8192 + 64, dst + 16384 + 1024);
  };
  auto piece_wx = [&](gptr wqh, gptr xbp, int c, int slot, int pc) {
    unsigned base = l0 + slot * SLOT;
    gptr ws_ = wqh + (size_t)c * (32 * 4096);
    if (pc == 0) glds(wg, ws_, base + w * 2048);
    if (pc == 1) glds(wg, ws_ + 64, base + w * 2048 + 1024);
    if (pc == 2) glds(xg, xbp + c * 32, base + 16384 + w * 1024);
  };
  auto issue_wx = [&](gptr wqh, gptr xbp, int c, int slot) {
    piece_wx(wqh, xbp, c, slot, 0);
    piece_wx(wqh, xbp, c, slot, 1);
    piece_wx(wqh, xbp, c, slot, 2);
  };

  const int q4 = (lane & 15) >> 2, p4 = lane & 3, blk = (lane >> 4) & 1;
  const int p4s = ((p4 & 1) << 1) | (p4 >> 1);
  const int wbase = h * 4096 + g * 2048 + q4 * 64 + blk * 32 + p4s * 8;
  const int xrow = 32 * tb + l31;
  const int xb0 = 16384 + 512 * (xrow >> 3) + 64 * (xrow & 7) + 16 * ((0 + h) ^ ((xrow >> 2) & 3));
  const int xb1 = 16384 + 512 * (xrow >> 3) + 64 * (xrow & 7) + 16 * ((2 + h) ^ ((xrow >> 2) & 3));
  const int kb0 = 2048 * (l31 >> 3) + 64 * (l31 & 7) + 16 * ((0 + h) ^ ((l31 >> 2) & 3));
  const int kb1 = 2048 * (l31 >> 3) + 64 * (l31 & 7) + 16 * ((2 + h) ^ ((l31 >> 2) & 3));
  const int vb = 16384 + h * 256 + q4 * 64 + blk * 32 + p4 * 8;

  int rs = 0, wsl = 4;
#define ADV(x) x = (x == 4 ? 0 : x + 1)
  struct T_ { constexpr operator bool() const { return true; } };
  struct F_ { constexpr operator bool() const { return false; } };
  {
    gptr wqh = wqA + hkv * 256, xbp = xA + (size_t)b * (128 * 576);
    issue_wx(wqh, xbp, 0, 0);
    issue_wx(wqh, xbp, 1, 1);
    issue_wx(wqh, xbp, 2, 2);
    issue_wx(wqh, xbp, 3, 3);
  }
  gptr nwq = wqB + hkv * 256, nx = xB + (size_t)b * (128 * 576);
  const char* wosrc = (const char*)((xcd >> 2) ? woB : woA) + (size_t)(xcd & 3) * (1024 * 1152) + (w * 32 + jj) * 4096;
  for (int item = 0; item < 2; ++item) {
    gptr wqh = item ? nwq : wqA + hkv * 256, xbp = item ? nx : xA + (size_t)b * (128 * 576);
    gptr Kh = (item ? kB : kA) + kvoff, Vh = (item ? vB : vA) + kvoff;
    f32x16 acc[4] = {};
    bf16x8 fa[10], fb[10];
    auto qstep = [&](bf16x8* Fn, bf16x8* Fc, auto domm, auto dma) {
      const char* base = shm + rs * SLOT;
      ADV(rs);
#pragma unroll
      for (int i = 0; i < 8; ++i) {
        if (domm) acc[i & 3] = MFMA32(Fc[i], Fc[8 + (i >> 2)], acc[i & 3]);
        if (i == 0) {
          Fn[8] = ld128(base + xb0);
          Fn[9] = ld128(base + xb1);
        }
        Fn[i] = tr2(base + wbase + (i >> 2) * 8192 + (i & 3) * 512, 256);
        if (i & 1) dma(i >> 1);
        SB();
      }
      ADV(wsl);
    };
    WAITBAR(9);
    qstep(fa, fb, F_{}, [&](int pc) { piece_wx(wqh, xbp, 4, wsl, pc); });
    for (int c = 0; c < 12; c += 2) {
      WAITBAR(9);
      qstep(fb, fa, T_{}, [&](int pc) { piece_wx(wqh, xbp, c + 5, wsl, pc); });
      WAITBAR(9);
      qstep(fa, fb, T_{}, [&](int pc) { piece_wx(wqh, xbp, c + 6, wsl, pc); });
    }
    WAITBAR(9);
    qstep(fb, fa, T_{}, [&](int pc) { piece_wx(wqh, xbp, 17, wsl, pc); });
    if (item == 0) {
      const int t = jj * 8 + w;
      if (t < 216) {
        const char* src = t < 144 ? (const char*)nwq + (t * 32768 - (jj & 1) * 512)
                                  : (const char*)nx + ((xcd * 72 + t - 144) * 4096 - b * 147456);
        touch(t < 144 ? (lane >> 4) * 8192 + (lane & 15) * 64 : lane * 64, src,
              l0 + (wsl == 0 ? 4 : wsl - 1) * SLOT + 24576 + w * 1024);
      }
    }
    for (int c = 0; c < 4; c += 2) {
      WAITBAR(12);
      qstep(fa, fb, T_{}, [&](int pc) { piece_kv(Kh, Vh, c, wsl, pc); });
      WAITBAR(12);
      qstep(fb, fa, T_{}, [&](int pc) { piece_kv(Kh, Vh, c + 1, wsl, pc); });
    }
    WAITBAR(9);
#pragma unroll
    for (int i = 0; i < 8; ++i) {
      acc[i & 3] = MFMA32(fb[i], fb[8 + (i >> 2)], acc[i & 3]);
      if (i & 1) piece_kv(Kh, Vh, 4, wsl, i >> 1);
      SB();
    }
    ADV(wsl);
    bf16x8 qf[4][2];
#pragma unroll
    for (int cc = 0; cc < 4; ++cc) {
      qf[cc][0] = pack8(acc[cc], 0, QSCALE);
      qf[cc][1] = pack8(acc[cc], 8, QSCALE);
    }

    f32x16 o[4] = {};
    float m, l = 0.f;
    f32x16 negm = {};
    f32x16 A0, A1, B0, B1;
    {
      const char* kp0 = shm + rs * SLOT + kb0;
      const char* kp1 = shm + rs * SLOT + kb1;
      bf16x8 kf[16];
#pragma unroll
      for (int i = 0; i < 16; ++i) kf[i] = ld128(((i & 2) ? kp1 : kp0) + (i & 1) * 8192 + (i >> 2) * 512);
#pragma unroll
      for (int i = 0; i < 16; ++i) {
        if (i & 1) A1 = MFMA32(kf[i], qf[i >> 2][(i >> 1) & 1], i < 2 ? negm : A1);
        else A0 = MFMA32(kf[i], qf[i >> 2][(i >> 1) & 1], i < 2 ? negm : A0);
      }
      float mx = fmaxf(A0[0], A1[0]);
#pragma unroll
      for (int r = 1; r < 16; ++r) mx = fmaxf(fmaxf(mx, A0[r]), A1[r]);
      m = fmaxf(mx, swap32(mx));
#pragma unroll
      for (int r = 0; r < 16; ++r) {
        A0[r] -= m;
        A1[r] -= m;
        negm[r] = -m;
      }
      SB();
    }
    auto astep = [&](f32x16& c0, f32x16& c1, f32x16& n0, f32x16& n1, auto doqk, auto dma) {
      const char* vbp = shm + rs * SLOT + vb;
      ADV(rs);
      const char* kp0 = shm + rs * SLOT + kb0;
      const char* kp1 = shm + rs * SLOT + kb1;
      bf16x8 kf[16], vf[16];
      u32x4 pw[4];
      if (doqk) {
#pragma unroll
        for (int i = 0; i < 4; ++i) kf[i] = ld128(((i & 2) ? kp1 : kp0) + (i & 1) * 8192 + (i >> 2) * 512);
      }
#pragma unroll
      for (int r = 0; r < 4; ++r) c0[r] = __builtin_amdgcn_exp2f(c0[r]);
      SB();
#pragma unroll
      for (int i = 0; i < 16; ++i) {
        if (doqk) {
          if (i & 1) n1 = MFMA32(kf[i], qf[i >> 2][(i >> 1) & 1], i < 2 ? negm : n1);
          else n0 = MFMA32(kf[i], qf[i >> 2][(i >> 1) & 1], i < 2 ? negm : n0);
          if (i < 12) kf[i + 4] = ld128((((i + 4) & 2) ? kp1 : kp0) + (i & 1) * 8192 + ((i + 4) >> 2) * 512);
        }
        if (i < 6) {
          c0[4 + 2 * i] = __builtin_amdgcn_exp2f(c0[4 + 2 * i]);
          c0[5 + 2 * i] = __builtin_amdgcn_exp2f(c0[5 + 2 * i]);
        } else if (i < 14) {
          c1[2 * i - 12] = __builtin_amdgcn_exp2f(c1[2 * i - 12]);
          c1[2 * i - 11] = __builtin_amdgcn_exp2f(c1[2 * i - 11]);
        }
        pw[i >> 2][i & 3] = i < 8 ? cvtpk(c0[2 * i], c0[2 * i + 1]) : cvtpk(c1[2 * i - 16], c1[2 * i - 15]);
        if (i >= 12) vf[i - 12] = tr2(vbp + (i - 12) * 512, 2048);
        SB();
      }
      float s0 = c0[0], s1 = c1[0], mx = 0.f;
#pragma unroll
      for (int i = 0; i < 16; ++i) {
        o[i & 3] = MFMA32(vf[i], __builtin_bit_cast(bf16x8, pw[i >> 2]), o[i & 3]);
        if (i < 12) vf[i + 4] = tr2(vbp + ((i + 4) >> 2) * 4096 + (i & 3) * 512, 2048);
        if (i > 0) {
          asm volatile("v_add_f32_e32 %0, %0, %1" : "+v"(s0) : "v"(c0[i]));
          asm volatile("v_add_f32_e32 %0, %0, %1" : "+v"(s1) : "v"(c1[i]));
        }
        if (doqk) {
          if (i == 1) mx = fmaxf(n0[0], n1[0]);
          if (i >= 1) mx = fmaxf(fmaxf(mx, n0[i]), n1[i]);
        }
        if ((i & 3) == 1) dma(i >> 2);
        SB();
      }
      ADV(wsl);
      l += s0 + s1;
      if (doqk) {
        if (__any(mx > THR)) {
          float rm = fmaxf(mx, swap32(mx));
          float delta = fmaxf(rm, 0.f);
          float alpha = __builtin_amdgcn_exp2f(-delta);
          m += delta;
          l *= alpha;
#pragma unroll
          for (int r = 0; r < 16; ++r) {
            n0[r] -= delta;
            n1[r] -= delta;
            negm[r] = -m;
          }
#pragma unroll
          for (int cc = 0; cc < 4; ++cc)
#pragma unroll
            for (int r = 0; r < 16; ++r) o[cc][r] *= alpha;
        }
      }
    };
    WAITBAR(12);
    wsl = wsl == 0 ? 4 : wsl - 1;
    astep(A0, A1, B0, B1, T_{}, [&](int) {});
    for (int j = 1; j < 11; j += 2) {
      WAITBAR(8);
      astep(B0, B1, A0, A1, T_{}, [&](int pc) { piece_kv(Kh, Vh, j + 4, wsl, pc); });
      WAITBAR(8);
      astep(A0, A1, B0, B1, T_{}, [&](int pc) { piece_kv(Kh, Vh, j + 5, wsl, pc); });
    }
    WAITBAR(8);
    astep(B0, B1, A0, A1, T_{}, [&](int pc) { piece_kv(Kh, Vh, 15, wsl, pc); });
    WAITBAR(8);
    astep(A0, A1, B0, B1, T_{}, [&](int pc) {
      if (item == 0) piece_wx(nwq, nx, 0, wsl, pc);
      else if (pc == 0 || (pc == 1 && w == 0))
        touch(lane * 64, wosrc + pc * (256 * 4096), l0 + wsl * SLOT + (w * 2 + pc) * 256);
    });
    WAITBAR(4);
    astep(B0, B1, A0, A1, T_{}, [&](int pc) { if (item == 0) piece_wx(nwq, nx, 1, wsl, pc); });
    WAITBAR(0);
    astep(A0, A1, B0, B1, T_{}, [&](int pc) { if (item == 0) piece_wx(nwq, nx, 2, wsl, pc); });
    WAITBAR(0);
    astep(B0, B1, A0, A1, F_{}, [&](int pc) { if (item == 0) piece_wx(nwq, nx, 3, wsl, pc); });
    l += swap32(l);
    const float inv = 1.0f / l;
    unsigned short* dst = ows + ((((size_t)(item * 16 + b) * 4 + tb) * 32 + (2 * hkv + g)) * 8) * 512 + lane * 8;
#pragma unroll
    for (int cc = 0; cc < 4; ++cc) {
      *reinterpret_cast<bf16x8*>(dst + (cc * 2 + 0) * 512) = pack8(o[cc], 0, inv);
      *reinterpret_cast<bf16x8*>(dst + (cc * 2 + 1) * 512) = pack8(o[cc], 8, inv);
    }
  }
  asm volatile("s_waitcnt vmcnt(0)" ::: "memory");
}

constexpr size_t PART_SCALES = (size_t)2048 * 9 * 64 * 4;
constexpr int ST2 = 26624;
#define MFMA16(a, b, c) __builtin_amdgcn_mfma_f32_16x16x32_bf16(a, b, c, 0, 0, 0)
__global__ __launch_bounds__(512, 2) void oproj_kernel(gptr ows, gptr woA, gptr woB, float* __restrict__ part) {
  extern __shared__ __attribute__((aligned(16))) char shm[];
  const int tid = threadIdx.x, lane = tid & 63;
  const int w = __builtin_amdgcn_readfirstlane(tid >> 6);
  const int tg = w >> 1, ch = w & 1;
  const int ii = blockIdx.x, xcd = ii & 7, jj = ii >> 3;
  const int prob = xcd >> 2, kq = xcd & 3, tbk = jj >> 1, cH = jj & 1;
  gptr wog = (prob ? woB : woA) + (size_t)(kq * 1024) * 576 + cH * 288;
  gptr og = ows + ((size_t)(prob * 64 + tbk * 4 + tg) * 128 + kq * 32) * 1024 + ch * 512;
  const unsigned ol = lane * 16;
  unsigned goff[3];
#pragma unroll
  for (int pc = 0; pc < 3; ++pc) {
    int L = (pc < 2 ? 2 * w + pc : 16 + (w & 1)) * 64 + lane, row = L / 36, sg = L % 36;
    goff[pc] = row * 1152 + (sg ^ (((row >> 2) & 1) << 1)) * 16;
  }
  const unsigned l0 = ldsaddr(shm);
  auto piece = [&](int st, int slot, int pc) {
    unsigned d = l0 + slot * ST2;
    if (pc == 0) glds(ol, og + (size_t)st * 1024, d + 18432 + w * 1024);
    else if (pc < 3) glds(goff[pc - 1], wog + (size_t)st * (32 * 576), d + (2 * w + pc - 1) * 1024);
    else if (w < 2) glds(goff[2], wog + (size_t)st * (32 * 576), d + (16 + w) * 1024);
  };
  const int gq = lane >> 4, g1 = gq & 1, q4 = (lane & 15) >> 2, p4 = lane & 3;
  const int abase = (16 * (gq >> 1) + 4 * g1 + q4) * 576 + ch * 288 + p4 * 8;
  const int a0 = abase + (ch ? -32 : 32) * g1, a1 = abase + (ch ? 32 : -32) * g1;
  const int ob = 18432 + tg * 2048 + (gq >> 1) * 1024 + (g1 * 32 + (lane & 15)) * 16;
  f32x4 acc[2][9] = {};
  bf16x8 fa[11], fb[11];
  int rs = 0;
  auto step = [&](bf16x8* Fn, bf16x8* Fc, auto domm, auto dma) {
    const char* s = shm + rs * ST2;
#pragma unroll
    for (int cb = 0; cb < 9; ++cb) {
      if (domm) {
        acc[0][cb] = MFMA16(Fc[cb], Fc[9], acc[0][cb]);
        acc[1][cb] = MFMA16(Fc[cb], Fc[10], acc[1][cb]);
      }
      if (cb == 0) {
        Fn[9] = ld128(s + ob);
        Fn[10] = ld128(s + ob + 256);
      }
      Fn[cb] = tr2(s + ((cb & 1) ? a1 : a0) + cb * 32, 4608);
      if (cb & 1) dma(cb >> 1);
      SB();
    }
    rs = rs == 5 ? 0 : rs + 1;
  };
  struct T_ { constexpr operator bool() const { return true; } };
  struct F_ { constexpr operator bool() const { return false; } };
#define WB2(n4, n3) do { if (w < 2) WAITBAR(n4); else WAITBAR(n3); } while (0)
  for (int st = 0; st < 5; ++st)
#pragma unroll
    for (int pc = 0; pc < 4; ++pc) piece(st, st, pc);
  WB2(16, 12);
  step(fa, fb, F_{}, [&](int pc) { piece(5, 5, pc); });
  for (int n = 1; n < 27; n += 2) {
    WB2(16, 12);
    step(fb, fa, T_{}, [&](int pc) { piece(n + 5, rs == 0 ? 5 : rs - 1, pc); });
    WB2(16, 12);
    step(fa, fb, T_{}, [&](int pc) { piece(n + 6, rs == 0 ? 5 : rs - 1, pc); });
  }
  WB2(16, 12);
  step(fb, fa, T_{}, [&](int) {});
  WB2(12, 9);
  step(fa, fb, T_{}, [&](int) {});
  WB2(8, 6);
  step(fb, fa, T_{}, [&](int) {});
  WB2(4, 3);
  step(fa, fb, T_{}, [&](int) {});
  WAITBAR(0);
  step(fb, fa, T_{}, [&](int) {});
#pragma unroll
  for (int cb = 0; cb < 9; ++cb) {
    acc[0][cb] = MFMA16(fb[cb], fb[9], acc[0][cb]);
    acc[1][cb] = MFMA16(fb[cb], fb[10], acc[1][cb]);
  }
  float m = 0.f;
#pragma unroll
  for (int u = 0; u < 2; ++u)
#pragma unroll
    for (int cb = 0; cb < 9; ++cb)
      m = fmaxf(fmaxf(m, fmaxf(fabsf(acc[u][cb][0]), fabsf(acc[u][cb][1]))),
                fmaxf(fabsf(acc[u][cb][2]), fabsf(acc[u][cb][3])));
  int ex = __builtin_amdgcn_frexp_expf(m);
  ex = ex < -100 ? -100 : ex;
  const float sc = __builtin_amdgcn_ldexpf(1.0f, 15 - ex);
  const size_t tw = (size_t)(((kq * 2 + prob) * 16 + tbk) * 2 + cH) * 8 + w;
  u32x4* dst = reinterpret_cast<u32x4*>(part) + tw * (9 * 64) + lane;
#pragma unroll
  for (int j = 0; j < 9; ++j) {
    const f32x4 a = acc[(2 * j) / 9][(2 * j) % 9], b = acc[(2 * j + 1) / 9][(2 * j + 1) % 9];
    u32x4 r = {cvtpkh(a[0] * sc, a[1] * sc), cvtpkh(a[2] * sc, a[3] * sc), cvtpkh(b[0] * sc, b[1] * sc),
               cvtpkh(b[2] * sc, b[3] * sc)};
    dst[j * 64] = r;
  }
  (part + PART_SCALES)[tw * 64 + lane] = __builtin_amdgcn_ldexpf(1.0f, ex - 15);
}

constexpr int RB = 3, TPB3 = 128;
__global__ __launch_bounds__(TPB3) void reduce_kernel(const float* __restrict__ part, unsigned short* __restrict__ out) {
  const int gid = blockIdx.x * TPB3 + threadIdx.x, lane = gid & 63;
  const int wv = __builtin_amdgcn_readfirstlane(gid >> 6);
  const int bi = wv % (9 / RB), tw = wv / (9 / RB), w = tw & 7, tile = tw >> 3;
  const int cH = tile & 1, tbk = (tile >> 1) & 15, prob = tile >> 5;
  const u32x4* p = reinterpret_cast<const u32x4*>(part) + ((size_t)tw * 9 + bi * RB) * 64 + lane;
  const float* sp = part + PART_SCALES + tw * 64 + lane;
  constexpr size_t KQ = (size_t)512 * 9 * 64, KQS = (size_t)512 * 64;
  u32x4 v[RB][4];
  float inv[4];
#pragma unroll
  for (int i = 0; i < RB; ++i)
#pragma unroll
    for (int k = 0; k < 4; ++k) v[i][k] = __builtin_nontemporal_load(p + i * 64 + k * KQ);
#pragma unroll
  for (int k = 0; k < 4; ++k) inv[k] = sp[k * KQS];
  SB();
#pragma unroll
  for (int i = 0; i < RB; ++i)
#pragma unroll
    for (int h = 0; h < 2; ++h) {
      const int blk = 2 * (bi * RB + i) + h, u = blk / 9, cb = blk % 9;
      float s[4];
#pragma unroll
      for (int e = 0; e < 4; ++e) {
        float t[4];
#pragma unroll
        for (int k = 0; k < 4; ++k) {
          const unsigned raw = v[i][k][2 * h + (e >> 1)];
          const f16x2 x = __builtin_bit_cast(f16x2, raw);
          t[k] = (float)x[e & 1] * inv[k];
        }
        s[e] = (t[0] + t[1]) + (t[2] + t[3]);
      }
      const int tok = tbk * 128 + (w >> 1) * 32 + u * 16 + (lane & 15);
      const int col = prob * 576 + cH * 288 + (w & 1) * 144 + cb * 16 + 4 * (lane >> 4);
      u32x2 r = {cvtpk(s[0], s[1]), cvtpk(s[2], s[3])};
      *reinterpret_cast<u32x2*>(out + (size_t)tok * 1152 + col) = r;
    }
}

extern "C" void kernel_launch(void* const* d_in, const int* in_sizes, int n_in, void* d_out, int out_size, void* d_ws,
                              size_t ws_size, hipStream_t stream) {
  (void)in_sizes; (void)n_in; (void)out_size; (void)ws_size;
  gptr xA = (gptr)d_in[0], kA = (gptr)d_in[1], vA = (gptr)d_in[2], wqA = (gptr)d_in[3], woA = (gptr)d_in[4];
  gptr xB = (gptr)d_in[5], kB = (gptr)d_in[6], vB = (gptr)d_in[7], wqB = (gptr)d_in[8], woB = (gptr)d_in[9];
  unsigned short* ows = (unsigned short*)d_ws;
  float* part = (float*)((char*)d_ws + ((size_t)1 << 25));
  hipFuncSetAttribute((const void*)attn_kernel, hipFuncAttributeMaxDynamicSharedMemorySize, 163840);
  hipFuncSetAttribute((const void*)oproj_kernel, hipFuncAttributeMaxDynamicSharedMemorySize, 163840);
  hipLaunchKernelGGL(attn_kernel, dim3(256), dim3(512), 163840, stream, xA, kA, vA, wqA, xB, kB, vB, wqB, woA, woB, ows);
  hipLaunchKernelGGL(oproj_kernel, dim3(256), dim3(512), 6 * ST2, stream, (gptr)ows, woA, woB, part);
  hipLaunchKernelGGL(reduce_kernel, dim3(512 * (9 / RB) * 64 / TPB3), dim3(TPB3), 0, stream, (const float*)part, (unsigned short*)d_out);
}
```
